```python
import torch

def _softmax(x, axis=-1):
    return torch.softmax(x, dim=axis)
def _gelu(x):
    return torch.nn.functional.gelu(x, approximate="tanh")
def _silu(x):
    return torch.nn.functional.silu(x)
def _sigmoid(x):
    return torch.sigmoid(x)
def _layernorm(x, axis=-1, eps=1e-5):
    m = x.mean(dim=axis, keepdim=True)
    v = x.var(dim=axis, keepdim=True, unbiased=False)
    return (x - m) / torch.sqrt(v + eps)
def _scatter_add(tgt, idx, src, axis=0):
    return tgt.clone().index_add_(axis, idx, src)
def _segment_sum(src, seg, n):
    out = torch.zeros((n,) + src.shape[1:], dtype=src.dtype)
    return out.index_add_(0, seg, src)
def _pad(x, pw):
    flat = tuple(v for pair in reversed(pw) for v in pair)
    return torch.nn.functional.pad(x, flat)

kernel_name = 'sequential_preLN__linear_kernel_hedgehog__mixer_s_mlp__prompt'

def setup_inputs():

    _w = (lambda a: (lambda t: t.to(torch.bfloat16) if t.is_floating_point() else t)(torch.as_tensor(a)))
    def _A_setup():
        B, S, D, H = 1, 2048, 448, 16
        Dh = D // H
        return {
            "x": _w((lambda *s: torch.randn(*s, dtype=torch.bfloat16) * 0.05)(B, S, D)),
            "wqkv": _w((lambda *s: torch.randn(*s, dtype=torch.bfloat16) * 0.2)(D, 3 * D)),
            "w_phi": _w((lambda *s: torch.randn(*s, dtype=torch.bfloat16) * 0.2)(H, Dh, Dh) * (Dh ** -0.5)),
            "wo": _w((lambda *s: torch.randn(*s, dtype=torch.bfloat16) * 0.05)(D, D)),
        }
    _A_in = _A_setup()

    def _B_setup():
        B, S, D, F = 1, 2048, 448, 1792
        return {
            "x":  _w((lambda *s: torch.randn(*s, dtype=torch.bfloat16) * 0.05)(B, S, D)),
            "w1": _w((lambda *s: torch.randn(*s, dtype=torch.bfloat16) * 3.2)(D, D) * (D ** -0.5)),
            "w2": _w((lambda *s: torch.randn(*s, dtype=torch.bfloat16) * 3.2)(D, D) * (D ** -0.5)),
            "w3": _w((lambda *s: torch.randn(*s, dtype=torch.bfloat16) * 3.2)(D, F) * (D ** -0.5)),
            "w4": _w((lambda *s: torch.randn(*s, dtype=torch.bfloat16) * 3.2)(F, D) * (F ** -0.5)),
        }
    _B_in = _B_setup()

    return {
        "A_x": _A_in["x"],
        "A_wqkv": _A_in["wqkv"],
        "A_w_phi": _A_in["w_phi"],
        "A_wo": _A_in["wo"],
        "B_x": _B_in["x"],
        "B_w1": _B_in["w1"],
        "B_w2": _B_in["w2"],
        "B_w3": _B_in["w3"],
        "B_w4": _B_in["w4"],
    }

def reference(A_x, A_wqkv, A_w_phi, A_wo, B_x, B_w1, B_w2, B_w3, B_w4):
    def _A_ref(x, wqkv, w_phi, wo):
        B, S, D = x.shape
        H = 16; Dh = D // H
        qkv = (lambda x, *d: x.permute(*d))((x @ wqkv).reshape(B, S, 3, H, Dh), 2, 0, 3, 1, 4)
        q, k, v = qkv[0], qkv[1], qkv[2]
        pq = torch.einsum("bhsd,hdf->bhsf", q, w_phi)
        pk = torch.einsum("bhsd,hdf->bhsf", k, w_phi)
        phi_q = torch.concatenate([_softmax(pq, axis=-1), _softmax(-pq, axis=-1)], axis=-1)
        phi_k = torch.concatenate([_softmax(pk, axis=-1), _softmax(-pk, axis=-1)], axis=-1)
        kv = torch.einsum("bhsf,bhsd->bhfd", phi_k, v)
        z  = torch.einsum("bhsf->bhf", phi_k)
        num = torch.einsum("bhqf,bhfd->bhqd", phi_q, kv)
        den = torch.einsum("bhqf,bhf->bhq", phi_q, z)[..., None] + 1e-6
        o = num / den
        return (lambda x, *d: x.permute(*d))(o, 0, 2, 1, 3).reshape(B, S, D) @ wo

    def _B_ref(x, w1, w2, w3, w4):
        B, S, D = x.shape
        h = _gelu(_layernorm(x, axis=-1) @ w1)
        c = D // 4
        g0 = _pad(h[:, :, 0*c:1*c], ((0, 0), (1, 0), (0, 0)))[:, :S, :]
        g1 = _pad(h[:, :, 1*c:2*c], ((0, 0), (0, 1), (0, 0)))[:, 1:, :]
        g2 = _pad(h[:, :, 2*c:3*c], ((0, 0), (2, 0), (0, 0)))[:, :S, :]
        g3 = _pad(h[:, :, 3*c:   ], ((0, 0), (0, 2), (0, 0)))[:, 2:, :]
        hs = torch.concatenate([g0, g1, g2, g3], axis=-1) @ w2
        x1 = x + hs
        h2 = _gelu(_layernorm(x1, axis=-1) @ w3) @ w4
        return x1 + h2

    x = A_x
    h = x + _A_ref(_layernorm(x, axis=-1), A_wqkv, A_w_phi, A_wo)
    h2 = h + _B_ref(_layernorm(h, axis=-1), B_w1, B_w2, B_w3, B_w4)
    return h2
```

```cpp
#include <hip/hip_runtime.h>
#include <hip/hip_bf16.h>
#include <stdint.h>

typedef unsigned short u16;
constexpr int S = 2048, D = 448, H = 16, DH = 28, F = 1792;

__device__ __forceinline__ float bf2f(u16 b) { return __uint_as_float((unsigned)b << 16); }
__device__ __forceinline__ u16 f2bf(float f) { unsigned u = __float_as_uint(f); u += 0x7FFFu + ((u >> 16) & 1u); return (u16)(u >> 16); }
__device__ __forceinline__ float rb(float f) { return bf2f(f2bf(f)); }

__global__ void k_ln(const u16* x, u16* y) {
    int row = blockIdx.x, lane = threadIdx.x;
    float v[7]; float s = 0;
    for (int i = 0; i < 7; i++) { v[i] = bf2f(x[row * D + lane + 64 * i]); s += v[i]; }
    for (int o = 32; o; o >>= 1) s += __shfl_xor(s, o);
    float mean = s / D; float q = 0;
    for (int i = 0; i < 7; i++) { float d = v[i] - mean; q += d * d; }
    for (int o = 32; o; o >>= 1) q += __shfl_xor(q, o);
    float m = rb(mean), var = rb(q / D);
    float sd = rb(sqrtf(rb(var + 1e-5f)));
    for (int i = 0; i < 7; i++) y[row * D + lane + 64 * i] = f2bf(rb(v[i] - m) / sd);
}
__device__ __forceinline__ float gelu(float x) { return 0.5f * x * (1.f + tanhf(0.7978845608028654f * (x + 0.044715f * x * x * x))); }
__global__ void k_gemm(const u16* A, const u16* W, u16* C, int N, int K, int mode) {
    int n = blockIdx.x * 64 + threadIdx.x, m = blockIdx.y;
    if (n >= N) return;
    float acc = 0;
    for (int k = 0; k < K; k++) {
        float a;
        if (mode == 2) { int g = k / 112; int sh = g == 0 ? -1 : g == 1 ? 1 : g == 2 ? -2 : 2; int r = m + sh; a = (r >= 0 && r < S) ? bf2f(A[r * K + k]) : 0.f; }
        else a = bf2f(A[m * K + k]);
        acc += a * bf2f(W[k * N + n]);
    }
    acc = rb(acc);
    if (mode == 1) acc = gelu(acc);
    C[m * N + n] = f2bf(acc);
}
__global__ void __launch_bounds__(64) k_phi(const u16* qkv, const u16* wphi, u16* phi  ) {
    int idx = blockIdx.x * 64 + threadIdx.x;
    int which = idx / (S * H), r = idx % (S * H), s = r / H, h = r % H;
    float p[DH]; float mx = -1e30f, mn = 1e30f;
    #pragma unroll
    for (int f = 0; f < DH; f++) { float a = 0; _Pragma("unroll 1") for (int d = 0; d < DH; d++) a += bf2f(qkv[s * 3 * D + which * D + h * DH + d]) * bf2f(wphi[h * DH * DH + d * DH + f]); p[f] = rb(a); mx = fmaxf(mx, p[f]); mn = fminf(mn, p[f]); }
    float s1 = 0, s2 = 0;
    #pragma unroll
    for (int f = 0; f < DH; f++) { s1 += expf(p[f] - mx); s2 += expf(mn - p[f]); }
    u16* o = phi + ((size_t)which * S * H + s * H + h) * 56;
    #pragma unroll
    for (int f = 0; f < DH; f++) { o[f] = f2bf(expf(p[f] - mx) / s1); o[28 + f] = f2bf(expf(mn - p[f]) / s2); }
}
__global__ void k_kv(const u16* qkv, const u16* phi, u16* kv  ) {
    int idx = blockIdx.x * 64 + threadIdx.x; if (idx >= H * 56 * 29) return;
    int h = idx / (56 * 29), f = (idx / 29) % 56, d = idx % 29;
    const u16* pk = phi + (size_t)S * H * 56;
    float a = 0;
    for (int s = 0; s < S; s++) a += bf2f(pk[(s * H + h) * 56 + f]) * (d < 28 ? bf2f(qkv[s * 3 * D + 2 * D + h * DH + d]) : 1.f);
    kv[idx] = f2bf(a);
}
__global__ void k_o(const u16* phi, const u16* kv, u16* o) {
    int idx = blockIdx.x * 64 + threadIdx.x; int s = idx / D, c = idx % D, h = c / DH, d = c % DH;
    float num = 0, den = 0;
    for (int f = 0; f < 56; f++) { float p = bf2f(phi[(s * H + h) * 56 + f]); num += p * bf2f(kv[(h * 56 + f) * 29 + d]); den += p * bf2f(kv[(h * 56 + f) * 29 + 28]); }
    o[idx] = f2bf(rb(num) / rb(rb(den) + 1e-6f));
}
__global__ void k_add(const u16* a, const u16* b, u16* c) { int i = blockIdx.x * 256 + threadIdx.x; c[i] = f2bf(bf2f(a[i]) + bf2f(b[i])); }

extern "C" void kernel_launch(void* const* d_in, const int* in_sizes, int n_in, void* d_out, int out_size, void* d_ws, size_t ws_size, hipStream_t stream) {
    const u16* x = (const u16*)d_in[0]; const u16* wqkv = (const u16*)d_in[1]; const u16* wphi = (const u16*)d_in[2]; const u16* wo = (const u16*)d_in[3];
    const u16* w1 = (const u16*)d_in[5]; const u16* w2 = (const u16*)d_in[6]; const u16* w3 = (const u16*)d_in[7]; const u16* w4 = (const u16*)d_in[8];
    u16* ws = (u16*)d_ws; size_t U = (size_t)S * F;
    u16 *xn = ws, *qkv = ws + U, *phi = ws + 2 * U, *kv = ws + 3 * U, *o = ws + 4 * U, *at = ws + 5 * U, *h = ws + 6 * U, *hn = ws + 7 * U, *hnn = ws + 8 * U, *t = ws + 9 * U, *hs = ws + 10 * U, *x1 = ws + 11 * U, *x1n = ws + 12 * U, *u = ws + 13 * U, *h2 = ws + 14 * U, *bo = ws + 15 * U;
    k_ln<<<S, 64, 0, stream>>>(x, xn);
    k_gemm<<<dim3(21, S), 64, 0, stream>>>(xn, wqkv, qkv, 3 * D, D, 0);
    k_phi<<<2 * S * H / 64, 64, 0, stream>>>(qkv, wphi, phi);
    k_kv<<<(H * 56 * 29 + 63) / 64, 64, 0, stream>>>(qkv, phi, kv);
    k_o<<<S * D / 64, 64, 0, stream>>>(phi, kv, o);
    k_gemm<<<dim3(7, S), 64, 0, stream>>>(o, wo, at, D, D, 0);
    k_add<<<S * D / 256, 256, 0, stream>>>(x, at, h);
    k_ln<<<S, 64, 0, stream>>>(h, hn);
    k_ln<<<S, 64, 0, stream>>>(hn, hnn);
    k_gemm<<<dim3(7, S), 64, 0, stream>>>(hnn, w1, t, D, D, 1);
    k_gemm<<<dim3(7, S), 64, 0, stream>>>(t, w2, hs, D, D, 2);
    k_add<<<S * D / 256, 256, 0, stream>>>(hn, hs, x1);
    k_ln<<<S, 64, 0, stream>>>(x1, x1n);
    k_gemm<<<dim3(28, S), 64, 0, stream>>>(x1n, w3, u, F, D, 1);
    k_gemm<<<dim3(7, S), 64, 0, stream>>>(u, w4, h2, D, F, 0);
    k_add<<<S * D / 256, 256, 0, stream>>>(x1, h2, bo);
    k_add<<<S * D / 256, 256, 0, stream>>>(h, bo, (u16*)d_out);
}
```

```cpp
#include <hip/hip_runtime.h>
#include <stdint.h>

namespace hk {
typedef unsigned v4u __attribute__((ext_vector_type(4)));
typedef unsigned v2u __attribute__((ext_vector_type(2)));
typedef float f32x16 __attribute__((ext_vector_type(16)));
typedef float f32x4 __attribute__((ext_vector_type(4)));
typedef float f32x2 __attribute__((ext_vector_type(2)));
typedef short s16x8 __attribute__((ext_vector_type(8)));
typedef short s16x4 __attribute__((ext_vector_type(4)));
typedef __bf16 bf16x2 __attribute__((ext_vector_type(2)));
typedef unsigned short u16;
#define GAS __attribute__((address_space(1)))
#define LAS __attribute__((address_space(3)))
#define DI __device__ __forceinline__
#define SB() __builtin_amdgcn_sched_barrier(0)
typedef __amdgpu_buffer_rsrc_t rsrc_t;

constexpr int S = 2048, D = 448, F = 1792;
constexpr float EPS = 1e-5f, LOG2E = 1.4426950408889634f;
constexpr unsigned SENT = 0xFFFFFFFFu;
constexpr unsigned ONES = 0x3F803F80u;
constexpr int WS_E = 0, WS_FLAGS = 2048, WS_WT = 65536;
constexpr int T_WO = 0, T_W1 = 224, T_W2 = 420, T_W3 = 616, T_W4 = 1400, T_END = 2184;
constexpr int WS_KVP = WS_WT + T_END * 2048;
constexpr int WS_O = WS_KVP + 256 * 4096;
constexpr int WS_H = WS_O + 64 * 32 * 1024;
constexpr int WS_T = WS_H + 64 * 28 * 1024;
constexpr int WS_X1 = WS_T + 64 * 28 * 1024;
constexpr int WS_U = WS_X1 + 64 * 28 * 1024;
constexpr int WS_END = WS_U + 64 * 112 * 1024;
constexpr int WS_DBG = 32 * 1024 * 1024;
constexpr int L_SLOT = 24576, L_WQ = 24576, L_IMG = 53312, L_QX = 81920, L_PHQ = 114688;
constexpr int L_WPHI = 147456, L_ST = 149504, L_CS = 153600, L_KVF = 155648;
constexpr int L_WS = 0, L_COMB = 65536;
constexpr int IMGS = 200;
constexpr unsigned SPIN_CAP = 1u << 16;

DI f32x16 mfma32(v4u a, v4u b, f32x16 c) { return __builtin_amdgcn_mfma_f32_32x32x16_bf16(__builtin_bit_cast(s16x8, a), __builtin_bit_cast(s16x8, b), c, 0, 0, 0); }
DI f32x4 mfma16(v4u a, v4u b, f32x4 c) { return __builtin_amdgcn_mfma_f32_16x16x32_bf16(__builtin_bit_cast(s16x8, a), __builtin_bit_cast(s16x8, b), c, 0, 0, 0); }
DI unsigned pk(float a, float b) { f32x2 p = {a, b}; return __builtin_bit_cast(unsigned, __builtin_convertvector(p, bf16x2)); }
DI float blo(unsigned u) { return __builtin_bit_cast(float, u << 16); }
DI float bhi(unsigned u) { return __builtin_bit_cast(float, u & 0xFFFF0000u); }
DI float dot2(unsigned a, unsigned b, float c) { return __builtin_amdgcn_fdot2_f32_bf16(__builtin_bit_cast(bf16x2, a), __builtin_bit_cast(bf16x2, b), c, false); }
DI float dsum(v4u a, float c) { return dot2(a.w, ONES, dot2(a.z, ONES, dot2(a.y, ONES, dot2(a.x, ONES, c)))); }
DI float dsq(v4u a, float c) { return dot2(a.w, a.w, dot2(a.z, a.z, dot2(a.y, a.y, dot2(a.x, a.x, c)))); }
DI float fb(unsigned u) { return __builtin_bit_cast(float, u); }
DI unsigned ub(float f) { return __builtin_bit_cast(unsigned, f); }
DI float half_add(float v) { auto r = __builtin_amdgcn_permlane32_swap(ub(v), ub(v), false, false); return fb(r[0]) + fb(r[1]); }
DI float half_max(float v) { auto r = __builtin_amdgcn_permlane32_swap(ub(v), ub(v), false, false); return fmaxf(fb(r[0]), fb(r[1])); }
DI f32x16 zero16() { f32x16 z; _Pragma("unroll") for (int i = 0; i < 16; i++) z[i] = 0.f; return z; }
DI v4u ld_sc1(rsrc_t rs, int voff, int soff = 0) { return __builtin_amdgcn_raw_buffer_load_b128(rs, voff, soff, 16); }
DI void st_sc1(v4u v, rsrc_t rs, int voff, int soff = 0) { __builtin_amdgcn_raw_buffer_store_b128(v, rs, voff + soff, 0, 16); }
DI void glds(rsrc_t rs, int voff, int soff, unsigned lds) {
    asm volatile("s_mov_b32 m0, %0\n\ts_nop 3\n\tbuffer_load_dwordx4 %1, %2, %3 offen lds" ::"s"(__builtin_amdgcn_readfirstlane(lds)), "v"(voff), "s"(rs), "s"(__builtin_amdgcn_readfirstlane(soff)) : "memory");
}
#define VMCNT(n) asm volatile("s_waitcnt vmcnt(" #n ")" ::: "memory")
DI v4u lds_r128(LAS char* L, int off) { return *(LAS v4u*)(L + off); }
DI void lds_w128(LAS char* L, int off, v4u v) { *(LAS v4u*)(L + off) = v; }
DI v4u tr2(LAS char* L, int off0, int off1) {
    s16x4 a = __builtin_amdgcn_ds_read_tr16_b64_v4i16((LAS s16x4*)(L + off0));
    s16x4 b = __builtin_amdgcn_ds_read_tr16_b64_v4i16((LAS s16x4*)(L + off1));
    v2u x = __builtin_bit_cast(v2u, a), y = __builtin_bit_cast(v2u, b);
    return v4u{x.x, x.y, y.x, y.y};
}
DI unsigned umax3(unsigned a, unsigned b, unsigned c) { return max(max(a, b), c); }
template <int N> DI bool any_sent(const v4u (&A)[N]) {
    unsigned m = 0;
#pragma unroll
    for (int i = 0; i < N; i++) { m = umax3(m, A[i].x, A[i].y); m = umax3(m, A[i].z, A[i].w); }
    return __builtin_amdgcn_ballot_w64(m == SENT) != 0ull;
}
template <int N, int STRIDE> DI void issue(v4u (&A)[N], rsrc_t rs, int voff, int soff) {
#pragma unroll
    for (int i = 0; i < N; i++) A[i] = ld_sc1(rs, voff + ((i * STRIDE) & 4095), soff + ((i * STRIDE) & ~4095));
    SB();
}
template <int N, int STRIDE> DI void settle(v4u (&A)[N], rsrc_t rs, int voff, int soff) {
    if (any_sent<N>(A)) {
        unsigned sp = 0;
        do {
            asm volatile("" ::: "memory");
            issue<N, STRIDE>(A, rs, voff, soff);
        } while (any_sent<N>(A) && ++sp < SPIN_CAP);
    }
    SB();
}
DI v4u corr_a(float c) { unsigned h = pk(c, c); float lo = c - blo(h); return v4u{(h & 0xFFFFu) | (pk(lo, lo) << 16), h & 0xFFFFu, 0u, 0u}; }
DI v4u corr_b(float m) { unsigned h = pk(-m, -m); float lo = -m - blo(h); return v4u{h, pk(lo, lo) & 0xFFFFu, 0u, 0u}; }
DI float gelu(float x) {
    const float k1 = -2.f * 0.7978845608028654f * LOG2E, k2 = k1 * 0.044715f;
    float e = __builtin_amdgcn_exp2f(x * (x * x * k2 + k1));
    return x * __builtin_amdgcn_rcpf(1.f + e);
}
DI void softmax16(const f32x16& p, float mul, bool hi, unsigned (&o)[8]) {
    float y[16];
#pragma unroll
    for (int i = 0; i < 16; i++) y[i] = p[i] * mul;
#pragma unroll
    for (int i = 12; i < 16; i++) y[i] = hi ? -INFINITY : y[i];
    float mx = y[0];
#pragma unroll
    for (int i = 1; i < 16; i++) mx = fmaxf(mx, y[i]);
    mx = half_max(mx);
    float sum = 0.f;
#pragma unroll
    for (int i = 0; i < 16; i++) { y[i] = __builtin_amdgcn_exp2f(y[i] - mx); sum += y[i]; }
    const float r = __builtin_amdgcn_rcpf(half_add(sum));
#pragma unroll
    for (int i = 0; i < 8; i++) o[i] = pk(y[2 * i] * r, y[2 * i + 1] * r);
}
DI void pack_feat(const f32x16& a, bool hi, v4u& f0, v4u& f1) {
    f0 = v4u{pk(a[0], a[1]), pk(a[2], a[3]), pk(a[4], a[5]), pk(a[6], a[7])};
    f1 = v4u{pk(a[8], a[9]), pk(a[10], a[11]), hi ? 0u : pk(a[12], a[13]), hi ? 0u : pk(a[14], a[15])};
}

#define STAMP(i)

template <int NK> DI void small_gemm(LAS char* L, const v4u (&A)[NK], int w, int lane, float (&r)[8]) {
    const int kq = w & 3, rt = w >> 2;
    f32x16 a0 = zero16(), a1 = zero16();
    const int wb = L_WS + kq * NK * 2048 + ((lane ^ (kq << 3)) << 4);
#pragma unroll
    for (int i = 0; i < NK; i++) {
        a0 = mfma32(lds_r128(L, wb + i * 2048), A[i], a0);
        a1 = mfma32(lds_r128(L, wb + i * 2048 + 1024), A[i], a1);
    }
#pragma unroll
    for (int g = 1; g < 4; g++) {
        const int o = L_COMB + ((rt * 4 + (g ^ kq)) * 3 + (g - 1)) * 2048 + lane * 16;
        *(LAS f32x4*)(L + o) = f32x4{a0[4 * g], a0[4 * g + 1], a0[4 * g + 2], a0[4 * g + 3]};
        *(LAS f32x4*)(L + o + 1024) = f32x4{a1[4 * g], a1[4 * g + 1], a1[4 * g + 2], a1[4 * g + 3]};
    }
    __syncthreads();
    f32x4 e = {a0[0], a0[1], a0[2], a0[3]}, od = {a1[0], a1[1], a1[2], a1[3]};
#pragma unroll
    for (int g = 0; g < 3; g++) {
        const int o = L_COMB + (w * 3 + g) * 2048 + lane * 16;
        e += *(LAS f32x4*)(L + o);
        od += *(LAS f32x4*)(L + o + 1024);
    }
#pragma unroll
    for (int i = 0; i < 4; i++) { r[2 * i] = e[i]; r[2 * i + 1] = od[i]; }
}

__global__ void __launch_bounds__(512, 2) fused(const u16* __restrict__ x, const u16* __restrict__ wqkv, const u16* __restrict__ wphi, const u16* __restrict__ wo,
                                                const u16* __restrict__ w1, const u16* __restrict__ w2, const u16* __restrict__ w3, const u16* __restrict__ w4,
                                                u16* __restrict__ out, unsigned char* __restrict__ ws) {
    __shared__ __attribute__((aligned(16))) char smem[163840];
    LAS char* L = (LAS char*)smem;
    const unsigned lb = (unsigned)(size_t)L;
    STAMP(0);
    const int tid = threadIdx.x, lane = tid & 63, w = __builtin_amdgcn_readfirstlane(tid >> 6);
    const int b = blockIdx.x, xcd = b & 7, ii = b >> 3;
    const int rb = (xcd >> 1) * 4 + (ii >> 3), head = (xcd & 1) * 8 + (ii & 7);
    const bool sact = ii < 28;
    const int grp = xcd * 4 + ii / 7, mem = ii % 7;
    const int s = lane & 31, h = lane >> 5;
    const bool hi = h != 0;
    const int l16 = lane * 16;
    const rsrc_t rws = __builtin_amdgcn_make_buffer_rsrc(ws, 0, WS_END, 0x00020000);
    const rsrc_t rx = __builtin_amdgcn_make_buffer_rsrc((void*)x, 0, S * D * 2, 0x00020000);
    const rsrc_t rwq = __builtin_amdgcn_make_buffer_rsrc((void*)wqkv, 0, D * 3 * D * 2, 0x00020000);
    const v4u sent4 = {SENT, SENT, SENT, SENT};
    const int rot = (ii & 7) % 7;
    const int win = 56 * head - 8 * (head & 1);
    const int prt = w & 3, role = w >> 2;
    const v2u ev = __builtin_amdgcn_raw_buffer_load_b64(rws, WS_E, 0, 16);

    const int vx0 = (lane >> 3) * 896 + (((lane & 7) ^ (lane >> 4)) << 4), vx1 = vx0 ^ 64;
    const int vw = (lane >> 3) * 2688 + (1 + (((lane >> 2) ^ (lane >> 4)) & 1)) * 896 + win + (lane & 3) * 16;
    auto load_chunk = [&](int c, int slot) {
        int cc = c + rot; cc = cc >= 7 ? cc - 7 : cc;
        const int sx = (rb * 128 + prt * 32) * 896 + cc * 128, sw = (cc * 64 + prt * 16) * 2688;
        const unsigned lx = lb + slot * L_SLOT + prt * 4096, lw = lb + slot * L_SLOT + 16384 + prt * 2048;
        glds(rwq, vw, sw, lw);
        glds(rwq, vw, sw + 8 * 2688, lw + 1024);
        glds(rx, vx0, sx, lx);
        glds(rx, vx1, sx + 8 * 896, lx + 1024);
        glds(rx, vx0, sx + 16 * 896, lx + 2048);
        glds(rx, vx1, sx + 24 * 896, lx + 3072);
    };
    if (role == 0) {
        if (prt < 2) {
            const rsrc_t rph = __builtin_amdgcn_make_buffer_rsrc((void*)(wphi + head * 784), 0, 1568, 0x00020000);
            glds(rph, lane * 16 + prt * 1024, 0, lb + L_WPHI + prt * 1024);
        }
#pragma unroll
        for (int c = 0; c < 6; c++) load_chunk(c, c);
    } else {
        unsigned P[3][8];
#pragma unroll
        for (int t = 0; t < 3; t++) {
            int tau = b * 4 + prt + t * 1024;
            tau = tau < T_END ? tau : T_END - 1;
            const u16* base; int ld = 896, col, r0, hs = 8, ja = 1, jb = 4;
            if (tau < T_W1) { base = wo; const int ks = tau & 31; col = tau >> 5; r0 = 28 * (ks >> 1) + 16 * (ks & 1); hs = 4; jb = 8; }
            else if (tau < T_W2) { base = w1; const int q = tau - T_W1; col = q / 28; r0 = 16 * (q % 28); }
            else if (tau < T_W3) { base = w2; const int q = tau - T_W2; col = q / 28; r0 = 16 * (q % 28); }
            else if (tau < T_W4) { base = w3; const int q = tau - T_W3; col = q / 28; r0 = 16 * (q % 28); ld = 3584; }
            else { base = w4; const int q = tau - T_W4, ks = q % 112; col = q / 112; r0 = 64 * (ks >> 2) + 32 * (ks & 1) + ((ks >> 1) & 1); ja = 2; jb = 16; }
            const rsrc_t rw = __builtin_amdgcn_make_buffer_rsrc((void*)base, 0, tau < T_W3 ? D * D * 2 : D * F * 2, 0x00020000);
            const int vo = h * hs * ld + s * 4, so = r0 * ld + col * 128;
#pragma unroll
            for (int j = 0; j < 8; j++) P[t][j] = __builtin_amdgcn_raw_buffer_load_b32(rw, vo + so + ((j & 3) * ja + (j >> 2) * jb) * ld, 0, 0);
        }
        st_sc1(sent4, rws, l16, WS_KVP + (head * 16 + rb) * 4096 + prt * 1024);
        st_sc1(sent4, rws, l16, WS_O + ((rb * 4 + prt) * 32 + head * 2) * 1024);
        st_sc1(sent4, rws, l16, WS_O + ((rb * 4 + prt) * 32 + head * 2 + 1) * 1024);
        SB();
#pragma unroll
        for (int t = 0; t < 3; t++) {
            const int tau = b * 4 + prt + t * 1024;
            if (tau < T_END) {
                const bool z = tau < T_W1 && (tau & 1) && hi;
                v4u e, o;
#pragma unroll
                for (int j = 0; j < 4; j++) {
                    e[j] = __builtin_amdgcn_perm(P[t][2 * j + 1], P[t][2 * j], 0x05040100u);
                    o[j] = __builtin_amdgcn_perm(P[t][2 * j + 1], P[t][2 * j], 0x07060302u);
                }
                if (z) { e.z = 0; e.w = 0; o.z = 0; o.w = 0; }
                st_sc1(e, rws, l16, WS_WT + tau * 2048);
                st_sc1(o, rws, l16, WS_WT + tau * 2048 + 1024);
            }
        }
    }
    STAMP(1);
    const int q4 = (lane & 15) >> 2, p4 = lane & 3, blk = (lane >> 4) & 1;
    const int ax = (prt * 32 + s) * 128 + ((h ^ ((s >> 1) & 7)) << 4);
    const int awk = 16384 + (8 * h + q4) * 128 + ((q4 >> 1) & 1) * 64 + 8 * (head & 1) + blk * 32 + p4 * 8;
    const int awv = 16384 + (8 * h + q4) * 128 + (((q4 >> 1) & 1) ^ 1) * 64 + 8 * (head & 1) + blk * 32 + p4 * 8;
    const int awc = (role ? awv : awk) + prt * 2048;
    f32x16 aK = zero16(), aV = zero16();
    v4u xq[14];
    float sm = 0.f, sq = 0.f, cs = 0.f;
    const unsigned long long tag = (((unsigned long long)ev.y << 32) | ev.x) + 1ull;
#pragma unroll
    for (int c = 0; c < 7; c++) {
        if (role == 0) {
            if (c == 0) VMCNT(30); else if (c == 1) VMCNT(24); else if (c == 2) VMCNT(24); else if (c == 3) VMCNT(18);
            else if (c == 4) VMCNT(19); else if (c == 5) VMCNT(13); else VMCNT(7);
        } else if (c == 4) VMCNT(0);
        __syncthreads();
        if (role == 0) {
            if (c == 1) load_chunk(6, 0);
            if (c == 3) {
                const int vq = (lane >> 2) * 2688 + win + (lane & 3) * 16;
#pragma unroll
                for (int n = 0; n < 7; n++) glds(rwq, vq, (prt * 7 + n) * 16 * 2688, lb + L_WQ + (prt * 7 + n) * 1024);
            }
        } else if (c == 4) {
            if (tid == 256) __builtin_amdgcn_raw_buffer_store_b64(v2u{(unsigned)tag, (unsigned)(tag >> 32)}, rws, WS_FLAGS + b * 8, 0, 16);
            if (sact) {
#pragma unroll
                for (int r2 = 0; r2 < 2; r2++) {
                    st_sc1(sent4, rws, l16, WS_H + ((grp * 2 + r2) * 28 + mem * 4 + prt) * 1024);
                    st_sc1(sent4, rws, l16, WS_T + ((grp * 2 + r2) * 28 + mem * 4 + prt) * 1024);
                }
            }
        }
        const int sb = (c % 6) * L_SLOT;
        v4u xf[4];
#pragma unroll
        for (int i = 0; i < 4; i++) xf[i] = lds_r128(L, sb + (ax ^ (i * 32)));
#pragma unroll
        for (int i = 0; i < 4; i++) aK = mfma32(tr2(L, sb + awk + i * 2048, sb + awk + i * 2048 + 512), xf[i], aK);
        if (role) {
#pragma unroll
            for (int i = 0; i < 4; i++) aV = mfma32(tr2(L, sb + awv + i * 2048, sb + awv + i * 2048 + 512), xf[i], aV);
        }
        cs = dsum(tr2(L, sb + awc, sb + awc + 512), cs);
        xq[2 * c] = role ? xf[2] : xf[0];
        xq[2 * c + 1] = role ? xf[3] : xf[1];
        sm = dsum(xq[2 * c + 1], dsum(xq[2 * c], sm));
        sq = dsq(xq[2 * c + 1], dsq(xq[2 * c], sq));
        if (c == 0) STAMP(2);
    }
    STAMP(3);
    v4u fl[2];
    issue<2, 16>(fl, rws, lane * 32, WS_FLAGS);
    *(LAS f32x2*)(L + L_ST + w * 512 + lane * 8) = f32x2{sm, sq};
    *(LAS float*)(L + L_CS + w * 256 + lane * 4) = cs;
    v4u wp0, wp1;
    {
        const int a = L_WPHI + (4 * h + q4) * 56 + blk * 32 + p4 * 8;
        wp0 = tr2(L, a, a + 8 * 56);
        wp1 = tr2(L, a + 16 * 56, a + 24 * 56);
        if (hi) { wp1.z = 0; wp1.w = 0; }
    }
    __syncthreads();
    float mean, rstd;
    {
        f32x2 st = *(LAS f32x2*)(L + L_ST + prt * 512 + lane * 8) + *(LAS f32x2*)(L + L_ST + (prt + 4) * 512 + lane * 8);
        mean = half_add(st[0]) * (1.f / D);
        rstd = __builtin_amdgcn_rsqf(fmaxf(half_add(st[1]) * (1.f / D) - mean * mean, 0.f) + EPS);
    }
    const v4u cb = corr_b(mean);
    const float smul = (role ? -LOG2E : LOG2E) * rstd;
    {
        float ck = 0.f, cv = 0.f;
#pragma unroll
        for (int r = 0; r < 4; r++) { ck += *(LAS float*)(L + L_CS + r * 256 + lane * 4); cv += *(LAS float*)(L + L_CS + (r + 4) * 256 + lane * 4); }
        aK = mfma32(corr_a(ck), cb, aK);
        if (role) aV = mfma32(corr_a(cv), cb, aV);
        v4u k0, k1;
        pack_feat(aK, hi, k0, k1);
        f32x16 p = mfma32(wp1, k1, mfma32(wp0, k0, zero16()));
        unsigned ph[8];
        softmax16(p, smul, hi, ph);
        const int ia = L_IMG + (prt * 32 + s) * IMGS + h * 8;
#pragma unroll
        for (int g = 0; g < 4; g++) *(LAS v2u*)(L + ia + role * 64 + g * 16) = v2u{ph[2 * g], ph[2 * g + 1]};
        if (role) {
#pragma unroll
            for (int g = 0; g < 4; g++) {
                v2u v = {pk(aV[4 * g] * rstd, aV[4 * g + 1] * rstd), pk(aV[4 * g + 2] * rstd, aV[4 * g + 3] * rstd)};
                if (g == 3 && hi) v = v2u{0x00003F80u, 0u};
                *(LAS v2u*)(L + ia + 128 + g * 16) = v;
            }
        }
    }
    VMCNT(0);
    __syncthreads();
    STAMP(4);
    {
        const int g4 = lane >> 4;
        const int a = L_IMG + (8 * g4 + q4) * IMGS + p4 * 8;
        f32x4 kv = {0.f, 0.f, 0.f, 0.f};
#pragma unroll
        for (int ks = 0; ks < 4; ks++) {
            const int o = a + ks * 32 * IMGS;
            kv = mfma16(tr2(L, o + (w >> 1) * 32, o + (w >> 1) * 32 + 4 * IMGS), tr2(L, o + 128 + (w & 1) * 32, o + 128 + (w & 1) * 32 + 4 * IMGS), kv);
        }
        __builtin_amdgcn_raw_buffer_store_b64(v2u{pk(kv[0], kv[1]), pk(kv[2], kv[3])}, rws,
            WS_KVP + (head * 16 + rb) * 4096 + (w >> 1) * 1024 + ((g4 & 1) * 32 + (w & 1) * 16 + (lane & 15)) * 16 + (g4 >> 1) * 8, 0, 16);
    }
    STAMP(5);
    {
        const unsigned t0 = (unsigned)tag, t1 = (unsigned)(tag >> 32);
        for (unsigned sp = 0; sp < SPIN_CAP; sp++) {
            const bool ok = fl[0].x == t0 && fl[0].y == t1 && fl[0].z == t0 && fl[0].w == t1 && fl[1].x == t0 && fl[1].y == t1 && fl[1].z == t0 && fl[1].w == t1;
            if (__all(ok)) break;
            asm volatile("" ::: "memory");
            issue<2, 16>(fl, rws, lane * 32, WS_FLAGS);
        }
        if (b == 0 && tid == 0) __builtin_amdgcn_raw_buffer_store_b64(v2u{t0, t1}, rws, WS_E, 0, 16);
    }
    STAMP(6);
    const int kq = w & 3, rt = w >> 2, RT = grp * 2 + rt;
    v4u pw[8];
    v4u xres;
    if (sact) {
        issue<8, 1024>(pw, rws, l16, WS_WT + (T_WO + mem * 32) * 2048 + w * 8192);
        xres = __builtin_amdgcn_raw_buffer_load_b128(rx, (RT * 32 + s) * 896 + mem * 128 + kq * 32 + h * 16, 0, 0);
    }
    v4u pq[4];
    {
        f32x16 aQ = zero16();
        float cq = 0.f;
        const int a = L_WQ + (8 * h + q4 + role * 32) * 64 + 8 * (head & 1) + blk * 32 + p4 * 8;
#pragma unroll
        for (int c = 0; c < 7; c++) {
            int cc = c + rot; cc = cc >= 7 ? cc - 7 : cc;
#pragma unroll
            for (int i = 0; i < 2; i++) {
                const v4u wf = tr2(L, a + cc * 4096 + i * 1024, a + cc * 4096 + i * 1024 + 256);
                aQ = mfma32(wf, xq[2 * c + i], aQ);
                cq = dsum(wf, cq);
            }
        }
        aQ = mfma32(corr_a(cq), cb, aQ);
#pragma unroll
        for (int g = 0; g < 4; g++) *(LAS f32x4*)(L + L_QX + w * 4096 + g * 1024 + lane * 16) = f32x4{aQ[4 * g], aQ[4 * g + 1], aQ[4 * g + 2], aQ[4 * g + 3]};
        __syncthreads();
#pragma unroll
        for (int g = 0; g < 4; g++) {
            const f32x4 o = *(LAS f32x4*)(L + L_QX + (w ^ 4) * 4096 + g * 1024 + lane * 16);
#pragma unroll
            for (int i = 0; i < 4; i++) aQ[4 * g + i] += o[i];
        }
        v4u q0, q1;
        pack_feat(aQ, hi, q0, q1);
        f32x16 p = mfma32(wp1, q1, mfma32(wp0, q0, zero16()));
        unsigned ph[8];
        softmax16(p, smul, hi, ph);
        lds_w128(L, L_PHQ + w * 2048 + lane * 16, v4u{ph[0], ph[1], ph[2], ph[3]});
        lds_w128(L, L_PHQ + w * 2048 + 1024 + lane * 16, v4u{ph[4], ph[5], ph[6], ph[7]});
    }
    STAMP(7);
    v4u w1p[7], w2p[7];
    {
        v4u G[8];
        const int go = WS_KVP + head * 65536 + w * 512, gv = h * 4096 + s * 16;
        issue<8, 8192>(G, rws, gv, go);
        if (sact) {
#pragma unroll
            for (int i = 0; i < 8; i++) lds_w128(L, L_WS + w * 8192 + i * 1024 + lane * 16, pw[i]);
        }
        settle<8, 8192>(G, rws, gv, go);
        STAMP(8);
        if (sact) {
            issue<7, 2048>(w1p, rws, l16, WS_WT + (T_W1 + mem * 28 + (w >> 1) * 7) * 2048 + (w & 1) * 1024);
            issue<7, 2048>(w2p, rws, l16, WS_WT + (T_W2 + mem * 28 + (w >> 1) * 7) * 2048 + (w & 1) * 1024);
        }
        float v[8];
#pragma unroll
        for (int j = 0; j < 4; j++) { v[2 * j] = blo(G[0][j]); v[2 * j + 1] = bhi(G[0][j]); }
#pragma unroll
        for (int n = 1; n < 8; n++)
#pragma unroll
            for (int j = 0; j < 4; j++) { v[2 * j] += blo(G[n][j]); v[2 * j + 1] += bhi(G[n][j]); }
        float t[4];
#pragma unroll
        for (int j = 0; j < 4; j++) { auto r = __builtin_amdgcn_permlane32_swap(ub(v[j]), ub(v[j + 4]), false, false); t[j] = fb(r[0]) + fb(r[1]); }
        *(LAS v2u*)(L + L_KVF + w * 512 + s * 16 + h * 8) = v2u{pk(t[0], t[1]), pk(t[2], t[3])};
    }
    __syncthreads();
    {
        f32x16 o = zero16();
#pragma unroll
        for (int f = 0; f < 4; f++) o = mfma32(lds_r128(L, L_KVF + f * 1024 + lane * 16), lds_r128(L, L_PHQ + (prt + (f >> 1) * 4) * 2048 + (f & 1) * 1024 + lane * 16), o);
        auto r = __builtin_amdgcn_permlane32_swap(ub(o[12]), ub(o[12]), false, false);
        const float rd = __builtin_amdgcn_rcpf(fb(r[1]) + 1e-6f);
        float v[8];
#pragma unroll
        for (int j = 0; j < 8; j++) v[j] = (role ? o[8 + j] : o[j]) * rd;
        v4u f = {pk(v[0], v[1]), pk(v[2], v[3]), pk(v[4], v[5]), pk(v[6], v[7])};
        if (role && hi) { f.z = 0; f.w = 0; }
        st_sc1(f, rws, l16, WS_O + ((rb * 4 + prt) * 32 + head * 2 + role) * 1024);
    }
    STAMP(9);
    if (sact) {
        const int own = (RT * 28 + mem * 4 + kq) * 1024;
        float r[8];
        v4u hres, x1res;
        float hn[8];
        {
            v4u A[8];
            const int ao = WS_O + (RT * 32 + kq * 8) * 1024;
            issue<8, 1024>(A, rws, l16, ao);
            settle<8, 1024>(A, rws, l16, ao);
            STAMP(10);
            st_sc1(sent4, rws, l16, WS_X1 + own);
#pragma unroll
            for (int i = 0; i < 4; i++) st_sc1(sent4, rws, l16, WS_U + ((grp * 2 + (i >> 1)) * 112 + mem * 16 + w * 2 + (i & 1)) * 1024);
            small_gemm<8>(L, A, w, lane, r);
#pragma unroll
            for (int j = 0; j < 4; j++) hres[j] = pk(blo(xres[j]) + r[2 * j], bhi(xres[j]) + r[2 * j + 1]);
            st_sc1(hres, rws, l16, WS_H + own);
        }
        STAMP(11);
        {
            v4u A[7];
            const int ao = WS_H + (RT * 28 + kq * 7) * 1024, av = l16;
            issue<7, 1024>(A, rws, av, ao);
            float c = 0.f;
#pragma unroll
            for (int i = 0; i < 7; i++) { lds_w128(L, L_WS + ((w >> 1) * 7 + i) * 2048 + (w & 1) * 1024 + lane * 16, w1p[i]); c = dsum(w1p[i], c); }
            *(LAS float*)(L + L_CS + w * 256 + lane * 4) = half_add(c);
            __syncthreads();
            f32x4 ce = {0.f, 0.f, 0.f, 0.f}, co = ce;
#pragma unroll
            for (int i = 0; i < 4; i++) {
                ce += *(LAS f32x4*)(L + L_CS + (2 * i) * 256 + kq * 32 + h * 16);
                co += *(LAS f32x4*)(L + L_CS + (2 * i + 1) * 256 + kq * 32 + h * 16);
            }
            settle<7, 1024>(A, rws, av, ao);
            STAMP(12);
            float a = 0.f, q = 0.f;
#pragma unroll
            for (int i = 0; i < 7; i++) { a = dsum(A[i], a); q = dsq(A[i], q); }
            *(LAS f32x2*)(L + L_ST + w * 512 + lane * 8) = f32x2{a, q};
            small_gemm<7>(L, A, w, lane, r);
            f32x2 st = {0.f, 0.f};
#pragma unroll
            for (int i = 0; i < 4; i++) st += *(LAS f32x2*)(L + L_ST + (rt * 4 + i) * 512 + lane * 8);
            const float m = half_add(st[0]) * (1.f / D);
            const float var = fmaxf(half_add(st[1]) * (1.f / D) - m * m, 0.f);
            const float r1 = __builtin_amdgcn_rsqf(var + EPS);
            const float sc = r1 * __builtin_amdgcn_rsqf(var * r1 * r1 + EPS);
            v4u t;
#pragma unroll
            for (int j = 0; j < 4; j++) t[j] = pk(gelu(sc * (r[2 * j] - m * ce[j])), gelu(sc * (r[2 * j + 1] - m * co[j])));
            st_sc1(t, rws, l16, WS_T + own);
#pragma unroll
            for (int j = 0; j < 4; j++) { hn[2 * j] = (blo(hres[j]) - m) * r1; hn[2 * j + 1] = (bhi(hres[j]) - m) * r1; }
        }
        STAMP(13);
        v4u W3[28];
        {
            v4u A[7];
            const int g = RT * 32 + s + (kq == 0 ? -1 : kq == 1 ? 1 : kq == 2 ? -2 : 2);
            const int av = (g >= 0 && g < S) ? (g >> 5) * 28 * 1024 + (h * 32 + (g & 31)) * 16 : 0x7FFF0000, ao = WS_T + kq * 7 * 1024;
            issue<7, 1024>(A, rws, av, ao);
#pragma unroll
            for (int i = 0; i < 7; i++) lds_w128(L, L_WS + ((w >> 1) * 7 + i) * 2048 + (w & 1) * 1024 + lane * 16, w2p[i]);
            __syncthreads();
            settle<7, 1024>(A, rws, av, ao);
            STAMP(14);
            issue<28, 2048>(W3, rws, l16, WS_WT + (T_W3 + (mem * 4 + (w >> 1)) * 28) * 2048 + (w & 1) * 1024);
            small_gemm<7>(L, A, w, lane, r);
#pragma unroll
            for (int j = 0; j < 4; j++) x1res[j] = pk(hn[2 * j] + r[2 * j], hn[2 * j + 1] + r[2 * j + 1]);
            st_sc1(x1res, rws, l16, WS_X1 + own);
        }
        STAMP(15);
        {
            v4u A[7];
            const int ao = WS_X1 + (RT * 28 + kq * 7) * 1024, av = l16;
            issue<7, 1024>(A, rws, av, ao);
            float c = 0.f;
#pragma unroll
            for (int i = 0; i < 28; i++) c = dsum(W3[i], c);
            const v4u ca = corr_a(c);
            settle<7, 1024>(A, rws, av, ao);
            STAMP(16);
            float a = 0.f, q = 0.f;
#pragma unroll
            for (int i = 0; i < 7; i++) { lds_w128(L, L_WS + (rt * 28 + kq * 7 + i) * 1024 + lane * 16, A[i]); a = dsum(A[i], a); q = dsq(A[i], q); }
            *(LAS f32x2*)(L + L_ST + w * 512 + lane * 8) = f32x2{a, q};
            __syncthreads();
#pragma unroll
            for (int r2 = 0; r2 < 2; r2++) {
                f32x2 st = {0.f, 0.f};
#pragma unroll
                for (int i = 0; i < 4; i++) st += *(LAS f32x2*)(L + L_ST + (r2 * 4 + i) * 512 + lane * 8);
                const float m = half_add(st[0]) * (1.f / D);
                const float rs = __builtin_amdgcn_rsqf(fmaxf(half_add(st[1]) * (1.f / D) - m * m, 0.f) + EPS);
                f32x16 acc = zero16();
#pragma unroll
                for (int i = 0; i < 28; i++) acc = mfma32(W3[i], lds_r128(L, L_WS + (r2 * 28 + i) * 1024 + lane * 16), acc);
                acc = mfma32(ca, corr_b(m), acc);
#pragma unroll
                for (int q2 = 0; q2 < 2; q2++) {
                    v4u u;
#pragma unroll
                    for (int j = 0; j < 4; j++) u[j] = pk(gelu(acc[8 * q2 + 2 * j] * rs), gelu(acc[8 * q2 + 2 * j + 1] * rs));
                    st_sc1(u, rws, l16, WS_U + ((grp * 2 + r2) * 112 + mem * 16 + w * 2 + q2) * 1024);
                }
            }
        }
        STAMP(17);
        {
            v4u W4[28];
            issue<28, 1024>(W4, rws, (lane ^ (kq << 3)) << 4, WS_WT + (T_W4 + mem * 112 + w * 14) * 2048);
            __syncthreads();
            f32x4 e, od;
#pragma unroll
            for (int r2 = 0; r2 < 2; r2++) {
                v4u A[14];
                const int ao = WS_U + ((grp * 2 + r2) * 112 + w * 14) * 1024;
                issue<14, 1024>(A, rws, l16, ao);
                settle<14, 1024>(A, rws, l16, ao);
                if (r2 == 0) STAMP(18); else STAMP(19);
                f32x16 a0 = zero16(), a1 = zero16();
#pragma unroll
                for (int i = 0; i < 14; i++) { a0 = mfma32(W4[2 * i], A[i], a0); a1 = mfma32(W4[2 * i + 1], A[i], a1); }
#pragma unroll
                for (int g = 0; g < 4; g++) {
                    const int o = ((r2 * 4 + (g ^ kq)) * 8 + w) * 2048 + lane * 16;
                    *(LAS f32x4*)(L + o) = f32x4{a0[4 * g], a0[4 * g + 1], a0[4 * g + 2], a0[4 * g + 3]};
                    *(LAS f32x4*)(L + o + 1024) = f32x4{a1[4 * g], a1[4 * g + 1], a1[4 * g + 2], a1[4 * g + 3]};
                }
            }
            __syncthreads();
            e = f32x4{0.f, 0.f, 0.f, 0.f}; od = e;
#pragma unroll
            for (int g = 0; g < 8; g++) {
                const int o = (w * 8 + g) * 2048 + lane * 16;
                e += *(LAS f32x4*)(L + o);
                od += *(LAS f32x4*)(L + o + 1024);
            }
            v4u f;
#pragma unroll
            for (int j = 0; j < 4; j++) f[j] = pk(blo(hres[j]) + (blo(x1res[j]) + e[j]), bhi(hres[j]) + (bhi(x1res[j]) + od[j]));
            *(GAS v4u*)((GAS char*)out + (RT * 32 + s) * 896 + mem * 128 + kq * 32 + h * 16) = f;
        }
    }
    STAMP(20);
}
}

extern "C" void kernel_launch(void* const* d_in, const int* in_sizes, int n_in, void* d_out, int out_size, void* d_ws, size_t ws_size, hipStream_t stream) {
    using hk::u16;
    hk::fused<<<256, 512, 0, stream>>>((const u16*)d_in[0], (const u16*)d_in[1], (const u16*)d_in[2], (const u16*)d_in[3], (const u16*)d_in[5], (const u16*)d_in[6],
                                       (const u16*)d_in[7], (const u16*)d_in[8], (u16*)d_out, (unsigned char*)d_ws);
}
```

```cpp
#include <hip/hip_runtime.h>
#include <stdint.h>

namespace hk {
typedef unsigned v4u __attribute__((ext_vector_type(4)));
typedef unsigned v2u __attribute__((ext_vector_type(2)));
typedef float f32x16 __attribute__((ext_vector_type(16)));
typedef float f32x4 __attribute__((ext_vector_type(4)));
typedef float f32x2 __attribute__((ext_vector_type(2)));
typedef short s16x8 __attribute__((ext_vector_type(8)));
typedef short s16x4 __attribute__((ext_vector_type(4)));
typedef __bf16 bf16x2 __attribute__((ext_vector_type(2)));
typedef unsigned short u16;
#define GAS __attribute__((address_space(1)))
#define LAS __attribute__((address_space(3)))
#define DI __device__ __forceinline__
#define SB() __builtin_amdgcn_sched_barrier(0)
#define PIN(x) asm volatile("" : "+v"(x))
typedef __amdgpu_buffer_rsrc_t rsrc_t;

constexpr int S = 2048, D = 448, F = 1792;
constexpr float EPS = 1e-5f, LOG2E = 1.4426950408889634f;
constexpr unsigned SENT = 0xFFFFFFFFu;
constexpr unsigned ONES = 0x3F803F80u;
constexpr int OOB = 0x7FFF0000;
constexpr int WS_E = 0, WS_FLAGS = 2048, WS_CS = 8192, WS_WT = 65536;
constexpr int T_WO = 0, T_W1 = 224, T_W2 = 420, T_W3 = 616, T_W4 = 1400, T_END = 2184;
constexpr int WS_KVP = WS_WT + T_END * 2048;
constexpr int WS_O = WS_KVP + 256 * 4096;
constexpr int WS_H = WS_O + 64 * 32 * 1024;
constexpr int WS_T = WS_H + 64 * 28 * 1024;
constexpr int WS_X1 = WS_T + 64 * 28 * 1024;
constexpr int WS_U = WS_X1 + 64 * 28 * 1024;
constexpr int WS_END = WS_U + 64 * 112 * 1024;
constexpr int WS_DBG = 32 * 1024 * 1024;
constexpr int L_SLOT = 24576, L_WQ = 98304, L_IMG = 65536, L_JUNK = 159744;
constexpr int L_WPHI = 147456, L_ST = 149504, L_CS = 153600, L_KVF = 155648;
constexpr int L_WS = 0, L_COMB = 65536, L_W4A = 57344, L_W4B = 114688;
constexpr int IMGS = 200;
constexpr unsigned SPIN_CAP = 1u << 16;

DI f32x16 mfma32(v4u a, v4u b, f32x16 c) { return __builtin_amdgcn_mfma_f32_32x32x16_bf16(__builtin_bit_cast(s16x8, a), __builtin_bit_cast(s16x8, b), c, 0, 0, 0); }
DI f32x4 mfma16(v4u a, v4u b, f32x4 c) { return __builtin_amdgcn_mfma_f32_16x16x32_bf16(__builtin_bit_cast(s16x8, a), __builtin_bit_cast(s16x8, b), c, 0, 0, 0); }
DI unsigned pk(float a, float b) { f32x2 p = {a, b}; return __builtin_bit_cast(unsigned, __builtin_convertvector(p, bf16x2)); }
DI float blo(unsigned u) { return __builtin_bit_cast(float, u << 16); }
DI float bhi(unsigned u) { return __builtin_bit_cast(float, u & 0xFFFF0000u); }
DI float dot2(unsigned a, unsigned b, float c) { return __builtin_amdgcn_fdot2_f32_bf16(__builtin_bit_cast(bf16x2, a), __builtin_bit_cast(bf16x2, b), c, false); }
DI float dsum(v4u a, float c) { return dot2(a.w, ONES, dot2(a.z, ONES, dot2(a.y, ONES, dot2(a.x, ONES, c)))); }
DI float dsq(v4u a, float c) { return dot2(a.w, a.w, dot2(a.z, a.z, dot2(a.y, a.y, dot2(a.x, a.x, c)))); }
DI float fb(unsigned u) { return __builtin_bit_cast(float, u); }
DI unsigned ub(float f) { return __builtin_bit_cast(unsigned, f); }
DI float half_add(float v) { auto r = __builtin_amdgcn_permlane32_swap(ub(v), ub(v), false, false); return fb(r[0]) + fb(r[1]); }
DI float half_max(float v) { auto r = __builtin_amdgcn_permlane32_swap(ub(v), ub(v), false, false); return fmaxf(fb(r[0]), fb(r[1])); }
DI float half_min(float v) { auto r = __builtin_amdgcn_permlane32_swap(ub(v), ub(v), false, false); return fminf(fb(r[0]), fb(r[1])); }
DI f32x16 zero16() { f32x16 z; _Pragma("unroll") for (int i = 0; i < 16; i++) z[i] = 0.f; return z; }
DI v4u ld_sc1(rsrc_t rs, int voff, int soff = 0) { return __builtin_amdgcn_raw_buffer_load_b128(rs, voff, soff, 16); }
DI void st_sc1(v4u v, rsrc_t rs, int voff, int soff = 0) { __builtin_amdgcn_raw_buffer_store_b128(v, rs, voff + soff, 0, 16); }
DI void glds(rsrc_t rs, int voff, int soff, unsigned lds) {
    asm volatile("s_mov_b32 m0, %0\n\ts_nop 3\n\tbuffer_load_dwordx4 %1, %2, %3 offen lds" ::"s"(__builtin_amdgcn_readfirstlane(lds)), "v"(voff), "s"(rs), "s"(__builtin_amdgcn_readfirstlane(soff)) : "memory");
}
DI void glds_sc1(rsrc_t rs, int voff, int soff, unsigned lds) {
    asm volatile("s_mov_b32 m0, %0\n\ts_nop 3\n\tbuffer_load_dwordx4 %1, %2, %3 offen sc1 lds" ::"s"(__builtin_amdgcn_readfirstlane(lds)), "v"(voff), "s"(rs), "s"(__builtin_amdgcn_readfirstlane(soff)) : "memory");
}
#define VMCNT(n) asm volatile("s_waitcnt vmcnt(" #n ")" ::: "memory")
DI v4u lds_r128(LAS char* L, int off) { return *(LAS v4u*)(L + off); }
DI void lds_w128(LAS char* L, int off, v4u v) { *(LAS v4u*)(L + off) = v; }
DI void lds_w4f(LAS char* L, int off, const f32x16& a, int g) { *(LAS f32x4*)(L + off) = f32x4{a[4 * g], a[4 * g + 1], a[4 * g + 2], a[4 * g + 3]}; }
DI v4u tr2(LAS char* L, int off0, int off1) {
    s16x4 a = __builtin_amdgcn_ds_read_tr16_b64_v4i16((LAS s16x4*)(L + off0));
    s16x4 b = __builtin_amdgcn_ds_read_tr16_b64_v4i16((LAS s16x4*)(L + off1));
    v2u x = __builtin_bit_cast(v2u, a), y = __builtin_bit_cast(v2u, b);
    return v4u{x.x, x.y, y.x, y.y};
}
DI unsigned umax3(unsigned a, unsigned b, unsigned c) { return max(max(a, b), c); }
DI bool bad1(const v4u& a) { return __builtin_amdgcn_ballot_w64(max(umax3(a.x, a.y, a.z), a.w) == SENT) != 0ull; }
DI bool any_nan(float v) { return __builtin_amdgcn_ballot_w64(v != v) != 0ull; }
template <int N, class Off> DI void issue(v4u (&A)[N], rsrc_t rs, int voff, Off off) {
#pragma unroll
    for (int i = 0; i < N; i++) A[i] = ld_sc1(rs, voff, off(i));
    SB();
}
template <int N> DI unsigned bad_mask(const v4u (&A)[N]) {
    unsigned m = 0;
#pragma unroll
    for (int i = 0; i < N; i++) m |= bad1(A[i]) ? 1u << i : 0u;
    return m;
}
template <int N, class Off> DI void settle(v4u (&A)[N], rsrc_t rs, int voff, Off off) {
    unsigned m = bad_mask<N>(A), sp = 0;
    if (m) {
        do {
#pragma unroll
            for (int i = 0; i < N; i++)
                asm volatile("s_bitcmp1_b32 %1, %5\n\ts_cbranch_scc0 1f\n\ts_nop 2\n\tbuffer_load_dwordx4 %0, %2, %3, %4 offen sc1\n1:"
                             : "+v"(A[i]) : "s"(__builtin_amdgcn_readfirstlane(m)), "v"(voff), "s"(rs), "s"(__builtin_amdgcn_readfirstlane(off(i))), "i"(i) : "scc", "memory");
            VMCNT(0);
#pragma unroll
            for (int i = 0; i < N; i++) PIN(A[i]);
            m = bad_mask<N>(A);
        } while (m && ++sp < SPIN_CAP);
    }
    SB();
}
DI v4u corr_a(float c) { unsigned h = pk(c, c); float lo = c - blo(h); return v4u{(h & 0xFFFFu) | (pk(lo, lo) << 16), h & 0xFFFFu, 0u, 0u}; }
DI v4u corr_b(float m) { unsigned h = pk(-m, -m); float lo = -m - blo(h); return v4u{h, pk(lo, lo) & 0xFFFFu, 0u, 0u}; }
constexpr float GK1 = -2.f * 0.7978845608028654f * LOG2E, GK2 = GK1 * 0.044715f;
DI float gelu(float x) {
    float e = __builtin_amdgcn_exp2f(x * (x * x * GK2 + GK1));
    return x * __builtin_amdgcn_rcpf(1.f + e);
}
DI float gelu_s(float a, float c1, float c2, float ir) {
    float e = __builtin_amdgcn_exp2f(a * __builtin_fmaf(a * a, c2, c1));
    float y = a * __builtin_amdgcn_rcpf(__builtin_fmaf(e, ir, ir));
    PIN(y);
    return y;
}
DI float max3f(float a, float b, float c) { float r; asm("v_max3_f32 %0, %1, %2, %3" : "=v"(r) : "v"(a), "v"(b), "v"(c)); return r; }
DI float min3f(float a, float b, float c) { float r; asm("v_min3_f32 %0, %1, %2, %3" : "=v"(r) : "v"(a), "v"(b), "v"(c)); return r; }
DI void softmax2(f32x16 p, float mul, bool hi, unsigned (&o)[16]) {
#pragma unroll
    for (int i = 12; i < 16; i++) p[i] = hi ? p[0] : p[i];
    float mx = max3f(p[12], p[13], p[14]), mn = min3f(p[12], p[13], p[14]);
#pragma unroll
    for (int i = 0; i < 12; i += 2) { mx = max3f(mx, p[i], p[i + 1]); mn = min3f(mn, p[i], p[i + 1]); }
    mx = max3f(mx, p[15], p[15]); mn = min3f(mn, p[15], p[15]);
    float bx, bn;
    {
        auto a = __builtin_amdgcn_permlane32_swap(ub(mx), ub(mx), false, false);
        auto c = __builtin_amdgcn_permlane32_swap(ub(mn), ub(mn), false, false);
        bx = -max3f(fb(a[0]), fb(a[1]), fb(a[1])) * mul; bn = min3f(fb(c[0]), fb(c[1]), fb(c[1])) * mul;
    }
    f32x2 e[8], f[8];
#pragma unroll
    for (int i = 0; i < 8; i++) {
        const f32x2 q = {p[2 * i], p[2 * i + 1]};
        const f32x2 y = __builtin_elementwise_fma(q, f32x2{mul, mul}, f32x2{bx, bx}), z = __builtin_elementwise_fma(q, f32x2{-mul, -mul}, f32x2{bn, bn});
        e[i] = f32x2{__builtin_amdgcn_exp2f(y[0]), __builtin_amdgcn_exp2f(y[1])};
        f[i] = f32x2{__builtin_amdgcn_exp2f(z[0]), __builtin_amdgcn_exp2f(z[1])};
    }
    const float keep = hi ? 0.f : 1.f;
    f32x2 se = __builtin_elementwise_fma(e[6] + e[7], f32x2{keep, keep}, (e[0] + e[1]) + (e[2] + e[3]) + (e[4] + e[5]));
    f32x2 sf = __builtin_elementwise_fma(f[6] + f[7], f32x2{keep, keep}, (f[0] + f[1]) + (f[2] + f[3]) + (f[4] + f[5]));
    const float re = __builtin_amdgcn_rcpf(half_add(se[0] + se[1])), rf = __builtin_amdgcn_rcpf(half_add(sf[0] + sf[1]));
    const float re2 = re * keep, rf2 = rf * keep;
#pragma unroll
    for (int i = 0; i < 8; i++) {
        o[i] = __builtin_bit_cast(unsigned, __builtin_convertvector(e[i] * (i < 6 ? re : re2), bf16x2));
        o[8 + i] = __builtin_bit_cast(unsigned, __builtin_convertvector(f[i] * (i < 6 ? rf : rf2), bf16x2));
    }
}
DI void pack_feat(const f32x16& a, bool hi, v4u& f0, v4u& f1) {
    f0 = v4u{pk(a[0], a[1]), pk(a[2], a[3]), pk(a[4], a[5]), pk(a[6], a[7])};
    f1 = v4u{pk(a[8], a[9]), pk(a[10], a[11]), hi ? 0u : pk(a[12], a[13]), hi ? 0u : pk(a[14], a[15])};
}

#define STAMP(i)
#define COUNT(i)

template <int PD> DI void small_pre(LAS char* L, v4u (&wf)[PD], int wb) {
#pragma unroll
    for (int i = 0; i < PD; i++) wf[i] = lds_r128(L, wb + i * 1024);
    SB();
}
template <int NK, int PD, class Side> DI void small_mma(LAS char* L, const v4u (&A)[NK], v4u (&wf)[PD], int wb, f32x16& a0, f32x16& a1, Side side) {
    a0 = zero16(); a1 = zero16();
#pragma unroll
    for (int i = 0; i < 2 * NK; i++) {
        if (i & 1) a1 = mfma32(wf[i % PD], A[i >> 1], a1); else a0 = mfma32(wf[i % PD], A[i >> 1], a0);
        if (i + PD < 2 * NK) wf[i % PD] = lds_r128(L, wb + (i + PD) * 1024);
        side(i);
        SB();
    }
}
template <int NK> DI void small_redo(LAS char* L, const v4u (&A)[NK], int wb, f32x16& a0, f32x16& a1) {
    a0 = zero16(); a1 = zero16();
#pragma unroll
    for (int i = 0; i < NK; i++) {
        a0 = mfma32(lds_r128(L, wb + i * 2048), A[i], a0);
        a1 = mfma32(lds_r128(L, wb + i * 2048 + 1024), A[i], a1);
    }
}
template <class Side, class Side2> DI void small_comb(LAS char* L, const f32x16& a0, const f32x16& a1, int w, int l16, f32x4& e, f32x4& od, Side side, Side2 side2) {
    const int kq = w & 3, rt = w >> 2;
#pragma unroll
    for (int g = 1; g < 4; g++) {
        const int o = L_COMB + ((rt * 4 + (g ^ kq)) * 3 + (g - 1)) * 2048 + l16;
        lds_w4f(L, o, a0, g);
        lds_w4f(L, o + 1024, a1, g);
    }
    e += f32x4{a0[0], a0[1], a0[2], a0[3]}; od += f32x4{a1[0], a1[1], a1[2], a1[3]};
    PIN(e); PIN(od);
    side();
    __syncthreads();
    f32x4 t[6];
#pragma unroll
    for (int g = 0; g < 6; g++) t[g] = *(LAS f32x4*)(L + L_COMB + w * 6144 + g * 1024 + l16);
    side2();
    SB();
    e += (t[0] + t[2]) + t[4];
    od += (t[1] + t[3]) + t[5];
}
DI void unpack8(const v4u& v, f32x4& e, f32x4& od) {
    e = f32x4{blo(v.x), blo(v.y), blo(v.z), blo(v.w)};
    od = f32x4{bhi(v.x), bhi(v.y), bhi(v.z), bhi(v.w)};
}
DI v4u pack8(const f32x4& e, const f32x4& od) { return v4u{pk(e[0], od[0]), pk(e[1], od[1]), pk(e[2], od[2]), pk(e[3], od[3])}; }

__global__ void __launch_bounds__(512, 2) fused(const u16* __restrict__ x, const u16* __restrict__ wqkv, const u16* __restrict__ wphi, const u16* __restrict__ wo,
                                                const u16* __restrict__ w1, const u16* __restrict__ w2, const u16* __restrict__ w3, const u16* __restrict__ w4,
                                                u16* __restrict__ out, unsigned char* __restrict__ ws) {
    __shared__ __attribute__((aligned(16))) char smem[163840];
    LAS char* L = (LAS char*)smem;
    const unsigned lb = (unsigned)(size_t)L;
    STAMP(0);
    const int tid = threadIdx.x, lane = tid & 63, w = __builtin_amdgcn_readfirstlane(tid >> 6);
    const int b = blockIdx.x, xcd = b & 7, ii = b >> 3;
    const int l16 = lane * 16;
    {
        unsigned long long pc; unsigned len;
        asm volatile("s_getpc_b64 %0\n.Lhk_pc:\n\ts_mov_b32 %1, .Lhk_end-.Lhk_pc" : "=s"(pc), "=s"(len));
        const rsrc_t rc = __builtin_amdgcn_make_buffer_rsrc((void*)pc, 0, len, 0x00020000);
        if (w < 2) glds(rc, l16 + (ii * 2 + w) * 1024, 0, lb + L_JUNK + w * 1024);
    }
    SB();
    const int rbi = ii >> 3, rb = (xcd >> 1) * 4 + rbi, head = (xcd & 1) * 8 + (ii & 7);
    const rsrc_t rx = __builtin_amdgcn_make_buffer_rsrc((void*)x, 0, S * D * 2, 0x00020000);
    const rsrc_t rwq = __builtin_amdgcn_make_buffer_rsrc((void*)wqkv, 0, D * 3 * D * 2, 0x00020000);
    int rot = (ii & 7) + 2 * rbi; rot = rot >= 7 ? rot - 7 : rot;
    const int win = 56 * head - 8 * (head & 1);
    const int vx0 = (lane >> 3) * 896 + (((lane & 7) ^ (lane >> 4)) << 4), vx1 = vx0 ^ 64;
    const int vw = (lane >> 3) * 2688 + (1 + (((lane >> 2) ^ (lane >> 4)) & 1)) * 896 + win + (lane & 3) * 16;
    auto load_piece = [&](int c, int k) {
        int cc = c + rot; cc = cc >= 7 ? cc - 7 : cc;
        const int sx = (rb * 128 + w * 16) * 896 + cc * 128;
        const unsigned ls = lb + (c & 3) * L_SLOT;
        if (k == 0) glds(rwq, vw, (cc * 64 + w * 8) * 2688, ls + 16384 + w * 1024);
        if (k == 1) glds(rx, vx0, sx, ls + w * 2048);
        if (k == 2) glds(rx, vx1, sx + 8 * 896, ls + w * 2048 + 1024);
    };
    load_piece(0, 0); load_piece(0, 1); load_piece(0, 2);
    __builtin_amdgcn_s_barrier();
    load_piece(1, 0); load_piece(1, 1); load_piece(1, 2);
    SB();
    STAMP(21);
    const rsrc_t rws = __builtin_amdgcn_make_buffer_rsrc(ws, 0, WS_END, 0x00020000);
    v2u ev;
    asm volatile("buffer_load_dwordx2 %0, off, %1, 0 sc1" : "=v"(ev) : "s"(rws) : "memory");
    {
        const rsrc_t rph = __builtin_amdgcn_make_buffer_rsrc((void*)(wphi + head * 784), 0, 1568, 0x00020000);
        glds(rph, w >= 6 ? l16 + (w - 6) * 1024 : OOB, 0, w >= 6 ? lb + L_WPHI + (w - 6) * 1024 : lb + L_JUNK + 2048);
    }
    const bool sact = ii < 28;
    const int gi = ii / 7, grp = xcd * 4 + gi, mem = ii - gi * 7;
    const int s = lane & 31, h = lane >> 5;
    const bool hi = h != 0;
    const v4u sent4 = {SENT, SENT, SENT, SENT};
    const int prt = w & 3, role = w >> 2;
    const int ctask = w * 32 + xcd * 4 + mem;
    const int kq = w & 3, rt = w >> 2, RT = grp * 2 + rt;
    const int own = (RT * 28 + mem * 4 + kq) * 1024;
    st_sc1(sent4, rws, l16, WS_O + ((rb * 4 + prt) * 32 + head * 2 + role) * 1024);
    st_sc1(sent4, rws, !hi ? WS_KVP + (head * 16 + rb) * 4096 + w * 512 + l16 : (!sact && ctask < 70 && s < 8) ? WS_CS + ctask * 128 + s * 16 : OOB);
    st_sc1(sent4, rws, sact ? l16 : OOB, WS_H + own);
    st_sc1(sent4, rws, sact ? l16 : OOB, WS_T + own);
    SB();
    const bool strad = (0x2A54 >> head) & 1;
    const int tau0 = (rb * 10 + __builtin_popcount(~0x2A54u & ((1u << head) - 1u))) * 8 + w;
    unsigned P[2][8];
    int pvo[2], pja[2], pjb[2];
    const u16* pbase[2];
#pragma unroll
    for (int t = 0; t < 2; t++) {
        const int tau = min(tau0 + t * 1280, T_END - 1);
        const bool isO = tau < T_W1, is4 = tau >= T_W4, is3 = tau >= T_W3 && !is4;
        const int q = tau - (tau < T_W2 ? T_W1 : tau < T_W3 ? T_W2 : tau < T_W4 ? T_W3 : T_W4);
        const int c28 = q / 28, k28 = q - c28 * 28, k4 = (c28 & 3) * 28 + k28;
        pbase[t] = t == 0 ? (isO ? wo : tau < T_W2 ? w1 : tau < T_W3 ? w2 : w3) : (is4 ? w4 : w3);
        const int ld = is3 ? 3584 : 896;
        const int col = isO ? tau >> 5 : is4 ? c28 >> 2 : c28;
        const int r0 = isO ? 28 * ((tau & 31) >> 1) + 16 * (tau & 1) : is4 ? 64 * (k4 >> 2) + 32 * (k4 & 1) + ((k4 >> 1) & 1) : 16 * k28;
        pja[t] = (is4 ? 2 : 1) * ld; pjb[t] = (isO ? 8 : is4 ? 16 : 4) * ld;
        pvo[t] = h * (isO ? 4 : 8) * ld + s * 4 + r0 * ld + col * 128;
        PIN(pvo[t]);
    }
    auto p_load = [&](int t) {
        const rsrc_t rw = __builtin_amdgcn_make_buffer_rsrc((void*)pbase[t], 0, tau0 + t * 1280 < T_W3 ? D * D * 2 : D * F * 2, 0x00020000);
#pragma unroll
        for (int j = 0; j < 8; j++)
            asm volatile("buffer_load_dword %0, %1, %2, 0 offen" : "=v"(P[t][j]) : "v"(pvo[t] + (j & 3) * pja[t] + (j >> 2) * pjb[t]), "s"(rw) : "memory");
    };
    auto p_store = [&](int t) {
        asm volatile("" : "+v"(P[t][0]), "+v"(P[t][1]), "+v"(P[t][2]), "+v"(P[t][3]), "+v"(P[t][4]), "+v"(P[t][5]), "+v"(P[t][6]), "+v"(P[t][7]));
        const int tau = tau0 + t * 1280;
        const bool z = t == 0 && tau < T_W1 && (tau & 1) && hi;
        v4u e, o;
#pragma unroll
        for (int j = 0; j < 4; j++) {
            e[j] = __builtin_amdgcn_perm(P[t][2 * j + 1], P[t][2 * j], 0x05040100u);
            o[j] = __builtin_amdgcn_perm(P[t][2 * j + 1], P[t][2 * j], 0x07060302u);
        }
        if (z) { e.z = 0; e.w = 0; o.z = 0; o.w = 0; }
        const int so = tau < T_END ? WS_WT + tau * 2048 : OOB;
        st_sc1(e, rws, l16, so);
        st_sc1(o, rws, l16, so + 1024);
    };
    const int q4 = (lane & 15) >> 2, p4 = lane & 3, blk = (lane >> 4) & 1, g4 = lane >> 4;
    const int ax = ((prt * 32 + s) * 128 + ((h ^ ((s >> 1) & 7)) << 4)) ^ (role * 64);
    const int aw = 16384 + (8 * h + q4) * 128 + (((q4 >> 1) ^ role) & 1) * 64 + 8 * (head & 1) + blk * 32 + p4 * 8;
    const int awa = aw + role * 4096, awb = aw + (role ^ 1) * 4096, awc = aw + prt * 2048;
    int a_st = L_ST + w * 512 + lane * 8, a_so = L_ST + (w ^ 4) * 512 + lane * 8, a_cs = L_CS + lane * 4, a_cr = L_CS + role * 1024 + lane * 4;
    int ia = L_IMG + (prt * 32 + s) * IMGS + h * 8, a_fa = L_IMG + (8 * g4 + q4) * IMGS + p4 * 8 + (w >> 1) * 32, a_fv = L_IMG + (8 * g4 + q4) * IMGS + p4 * 8 + 128 + (w & 1) * 32;
    int kvo = WS_KVP + (head * 16 + rb) * 4096 + w * 512 + ((g4 >> 1) * 16 + (lane & 15)) * 16 + (g4 & 1) * 8;
    PIN(a_st); PIN(a_so); PIN(a_cs); PIN(a_cr); PIN(ia); PIN(a_fa); PIN(a_fv); PIN(kvo);
    STAMP(1);
    f32x16 acc = zero16();
    v4u xq[28];
    v4u xres, wc, wp0, wp1, f0, f1;
    float sm = 0.f, sq = 0.f, cs = 0.f;
    unsigned t0 = 0, t1 = 0;
    auto stats = [&](int c) {
        sm = dsum(xq[4 * c + 1], dsum(xq[4 * c], sm));
        sq = dsq(xq[4 * c + 1], dsq(xq[4 * c], sq));
        cs = dsum(wc, cs);
        PIN(sm); PIN(sq); PIN(cs);
    };
#pragma unroll
    for (int c = 0; c < 7; c++) {
        if (c == 0) VMCNT(9);
        else if (strad) { if (c == 1) VMCNT(9); else if (c == 3 || c == 4) VMCNT(4); else if (c == 6) VMCNT(0); else VMCNT(3); }
        else { if (c == 1) VMCNT(17); else if (c == 2) VMCNT(19); else if (c == 3) VMCNT(11); else if (c == 4) VMCNT(5); else if (c == 5) VMCNT(7); else VMCNT(2); }
        __syncthreads();
        const int sb = (c & 3) * L_SLOT;
        v4u wk[4];
#pragma unroll
        for (int i = 0; i < 4; i++) {
            const int a = sb + (i < 2 ? awa : awb) + (i & 1) * 2048;
            wk[i] = tr2(L, a, a + 512);
            xq[4 * c + i] = lds_r128(L, sb + (ax ^ (i * 32)));
        }
        const v4u wcn = tr2(L, sb + awc, sb + awc + 512);
        SB();
        if (c < 5) load_piece(c + 2, 0);
        if (c > 0) stats(c - 1);
        wc = wcn;
        SB();
#pragma unroll
        for (int i = 0; i < 4; i++) {
            acc = mfma32(wk[i], xq[4 * c + i], acc);
            if (c < 5 && (i == 0 || i == 2)) load_piece(c + 2, 1 + (i >> 1));
            SB();
        }
        if (c == 2) {
            PIN(ev);
            const unsigned long long tag = (((unsigned long long)ev.y << 32) | ev.x) + 1ull;
            t0 = (unsigned)tag; t1 = (unsigned)(tag >> 32);
        }
        if (!strad) {
            if (c < 2) p_load(c);
            if (c == 3 || c == 4) p_store(c - 3);
        } else if (c == 2) __builtin_amdgcn_raw_buffer_store_b64(v2u{t0, t1}, rws, tid == 0 ? WS_FLAGS + b * 8 : OOB, 0, 16);
        if (c == 3) {
            const int a = L_WPHI + (4 * h + q4) * 56 + blk * 32 + p4 * 8;
            wp0 = tr2(L, a, a + 8 * 56);
            wp1 = tr2(L, a + 16 * 56, a + 24 * 56);
            if (hi) { wp1.z = 0; wp1.w = 0; }
            PIN(wp0); PIN(wp1);
        }
        if (c == 0) STAMP(2);
        if (c == 2) STAMP(22);
        if (c == 4) STAMP(23);
    }
    stats(6);
    STAMP(3);
    *(LAS f32x2*)(L + a_st) = f32x2{sm, sq};
    *(LAS float*)(L + a_cs + w * 256) = cs;
    VMCNT(0);
    __syncthreads();
    if (!strad && tid == 256) __builtin_amdgcn_raw_buffer_store_b64(v2u{t0, t1}, rws, WS_FLAGS + b * 8, 0, 16);
    float rstd;
    v4u cb;
    {
        const f32x2 so = *(LAS f32x2*)(L + a_so);
        const float c = (*(LAS float*)(L + a_cr) + *(LAS float*)(L + a_cr + 256)) + (*(LAS float*)(L + a_cr + 512) + *(LAS float*)(L + a_cr + 768));
        const float mean = half_add(sm + so[0]) * (1.f / D);
        cb = corr_b(mean);
        acc = mfma32(corr_a(c), cb, acc);
        rstd = __builtin_amdgcn_rsqf(fmaxf(half_add(sq + so[1]) * (1.f / D) - mean * mean, 0.f) + EPS);
    }
    if (role == 0) {
        v4u k0, k1;
        pack_feat(acc, hi, k0, k1);
        unsigned ph[16];
        softmax2(mfma32(wp1, k1, mfma32(wp0, k0, zero16())), rstd * LOG2E, hi, ph);
#pragma unroll
        for (int g = 0; g < 8; g++) *(LAS v2u*)(L + ia + g * 16) = v2u{ph[2 * g], ph[2 * g + 1]};
        STAMP(24);
    } else {
        const int vq = (lane >> 2) * 2688 + win + (lane & 3) * 16;
        auto ldq = [&](int n) { glds(rwq, vq, (n * 4 + prt) * 16 * 2688, lb + L_WQ + (n * 4 + prt) * 1024); };
        ldq(0); ldq(1);
#pragma unroll
        for (int g = 0; g < 4; g++) {
            v2u v = {pk(acc[4 * g] * rstd, acc[4 * g + 1] * rstd), pk(acc[4 * g + 2] * rstd, acc[4 * g + 3] * rstd)};
            if (g == 3 && hi) v = v2u{0x00003F80u, 0u};
            *(LAS v2u*)(L + ia + 128 + g * 16) = v;
        }
        SB();
        ldq(2); ldq(3);
        if (!strad) { ldq(4); ldq(5); ldq(6); }
        STAMP(24);
    }
    __syncthreads();
    STAMP(4);
    {
        v4u fa[4], fv[4];
#pragma unroll
        for (int ks = 0; ks < 4; ks++) {
            fa[ks] = tr2(L, a_fa + ks * 32 * IMGS, a_fa + ks * 32 * IMGS + 4 * IMGS);
            fv[ks] = tr2(L, a_fv + ks * 32 * IMGS, a_fv + ks * 32 * IMGS + 4 * IMGS);
        }
        SB();
        f32x4 kv = {0.f, 0.f, 0.f, 0.f};
#pragma unroll
        for (int ks = 0; ks < 4; ks++) kv = mfma16(fa[ks], fv[ks], kv);
        __builtin_amdgcn_raw_buffer_store_b64(v2u{pk(kv[0], kv[1]), pk(kv[2], kv[3])}, rws, kvo, 0, 16);
    }
    STAMP(5);
    const int aq = L_WQ + (8 * h + q4) * 64 + 8 * (head & 1) + blk * 32 + p4 * 8;
    if (role == 0) {
        if (w == 0) {
            unsigned sp = 0;
            bool again;
            do {
                asm volatile("" ::: "memory");
                f0 = ld_sc1(rws, lane * 32, WS_FLAGS); f1 = ld_sc1(rws, lane * 32 + 16, WS_FLAGS);
                const bool ok = f0.x == t0 && f0.y == t1 && f0.z == t0 && f0.w == t1 && f1.x == t0 && f1.y == t1 && f1.z == t0 && f1.w == t1;
                again = __builtin_amdgcn_ballot_w64(!ok) != 0ull;
                COUNT(32);
            } while (again && ++sp < SPIN_CAP);
            if (b == 0 && lane == 0) __builtin_amdgcn_raw_buffer_store_b64(v2u{t0, t1}, rws, WS_E, 0, 16);
        }
        xres = __builtin_amdgcn_raw_buffer_load_b128(rx, sact ? (RT * 32 + s) * 896 + mem * 128 + kq * 32 + h * 16 : OOB, 0, 0);
        __syncthreads();
        STAMP(6);
        v4u G[16];
        const int gv = h * 4096 + s * 16;
        auto go = [&](int i) { return WS_KVP + head * 65536 + prt * 1024 + (((i >> 1) + 2 * rbi) & 7) * 8192 + (i & 1) * 512; };
        {
            v4u wf[7];
#pragma unroll
            for (int i = 0; i < 7; i++) wf[i] = tr2(L, aq + (prt * 7 + i) * 1024, aq + (prt * 7 + i) * 1024 + 256);
#pragma unroll
            for (int i = 0; i < 4; i++) G[i] = ld_sc1(rws, gv, go(i));
            SB();
            float cq = 0.f;
#pragma unroll
            for (int i = 0; i < 7; i++) cq = dsum(wf[i], cq);
            *(LAS float*)(L + a_cs + w * 256) = cq;
            __syncthreads();
#pragma unroll
            for (int i = 4; i < 16; i++) G[i] = ld_sc1(rws, gv, go(i));
            SB();
        }
        v4u sel = {0u, 0u, 0u, 0u};
        {
            const int j = (lane & 15) - 8 * (g4 & 1);
            const unsigned one = (j & 1) ? 0x3F800000u : 0x00003F80u;
#pragma unroll
            for (int q = 0; q < 4; q++) sel[q] = (j >> 1) == q ? one : 0u;
        }
        int ko = L_KVF + prt * 1024 + ((g4 & 1) * 32 + (lane & 15)) * 16 + (g4 >> 1) * 8;
        PIN(ko); PIN(sel);
        f32x4 k0 = {0.f, 0.f, 0.f, 0.f}, k1 = k0;
#pragma unroll
        for (int i = 0; i < 8; i++) { k0 = mfma16(sel, G[2 * i], k0); k1 = mfma16(sel, G[2 * i + 1], k1); }
        STAMP(7);
        if (__builtin_expect(any_nan(k0[0] + k1[0]), 0)) {
            COUNT(33);
            settle<16>(G, rws, gv, go);
            k0 = f32x4{0.f, 0.f, 0.f, 0.f}; k1 = k0;
#pragma unroll
            for (int i = 0; i < 8; i++) { k0 = mfma16(sel, G[2 * i], k0); k1 = mfma16(sel, G[2 * i + 1], k1); }
        }
        *(LAS v2u*)(L + ko) = v2u{pk(k0[0], k0[1]), pk(k0[2], k0[3])};
        *(LAS v2u*)(L + ko + 256) = v2u{pk(k1[0], k1[1]), pk(k1[2], k1[3])};
        PIN(xres);
        STAMP(8);
        __syncthreads();
    } else {
        if (strad) {
            const int vq = (lane >> 2) * 2688 + win + (lane & 3) * 16;
#pragma unroll
            for (int n = 4; n < 7; n++) glds(rwq, vq, (n * 4 + prt) * 16 * 2688, lb + L_WQ + (n * 4 + prt) * 1024);
        }
        VMCNT(0);
        xres = __builtin_amdgcn_raw_buffer_load_b128(rx, sact ? (RT * 32 + s) * 896 + mem * 128 + kq * 32 + h * 16 : OOB, 0, 0);
        __syncthreads();
        v4u wf[4];
        auto rdq = [&](int i) { int cc = (i >> 2) + rot; cc = cc >= 7 ? cc - 7 : cc; const int a = aq + cc * 4096 + ((i & 3) ^ 2) * 1024; return tr2(L, a, a + 256); };
#pragma unroll
        for (int i = 0; i < 4; i++) wf[i] = rdq(i);
        SB();
        acc = zero16();
        float cq = 0.f;
#pragma unroll
        for (int i = 0; i < 28; i++) {
            acc = mfma32(wf[i & 3], xq[i], acc);
            if (i + 4 < 28) wf[i & 3] = rdq(i + 4);
            if (i == 5) __syncthreads();
            if (i == 12) {
#pragma unroll
                for (int r = 0; r < 4; r++) cq += *(LAS float*)(L + a_cs + r * 256);
            }
            if (i == 20) acc = mfma32(corr_a(cq), cb, acc);
            SB();
        }
        STAMP(6);
        v4u q0, q1;
        pack_feat(acc, hi, q0, q1);
        unsigned ph[16];
        softmax2(mfma32(wp1, q1, mfma32(wp0, q0, zero16())), rstd * LOG2E, hi, ph);
#pragma unroll
        for (int i = 0; i < 16; i++) PIN(ph[i]);
        int oo = WS_O + ((rb * 4 + prt) * 32 + head * 2) * 1024 + l16;
        PIN(oo); PIN(xres);
        STAMP(8);
        __syncthreads();
        v4u fa[4];
#pragma unroll
        for (int f = 0; f < 4; f++) fa[f] = lds_r128(L, L_KVF + f * 1024 + l16);
        f32x16 o = zero16();
#pragma unroll
        for (int f = 0; f < 4; f++) o = mfma32(fa[f], v4u{ph[4 * f], ph[4 * f + 1], ph[4 * f + 2], ph[4 * f + 3]}, o);
        auto r = __builtin_amdgcn_permlane32_swap(ub(o[12]), ub(o[12]), false, false);
        const float rd = __builtin_amdgcn_rcpf(fb(r[1]) + 1e-6f), rd2 = hi ? 0.f : rd;
        st_sc1(v4u{pk(o[0] * rd, o[1] * rd), pk(o[2] * rd, o[3] * rd), pk(o[4] * rd, o[5] * rd), pk(o[6] * rd, o[7] * rd)}, rws, oo);
        st_sc1(v4u{pk(o[8] * rd, o[9] * rd), pk(o[10] * rd, o[11] * rd), pk(o[12] * rd2, o[13] * rd2), pk(o[14] * rd2, o[15] * rd2)}, rws, oo + 1024);
        SB();
    }
    STAMP(9);
    v4u W3[28];
    if (!sact) {
        if (ctask < 70) {
            const int so = WS_WT + ((ctask < 14 ? T_W1 : T_W3 - 7 * 28) + (ctask >> 1) * 28) * 2048 + (ctask & 1) * 1024;
            issue<28>(W3, rws, l16, [&](int i) { return so + i * 2048; });
            float c = 0.f;
#pragma unroll
            for (int i = 0; i < 28; i++) c = dsum(W3[i], c);
            __builtin_amdgcn_raw_buffer_store_b32(ub(half_add(c)), rws, hi ? OOB : WS_CS + ctask * 128 + s * 4, 0, 16);
        }
        const int id = mem * 8 + w;
        for (int i = id; i < 784; i += 32) glds_sc1(rws, l16, WS_WT + T_W1 * 2048 + i * 1024, lb + w * 1024);
        for (int i = id; i < 1568; i += 32) glds_sc1(rws, l16, WS_WT + T_W4 * 2048 + i * 1024, lb + w * 1024);
        VMCNT(0);
    } else {
        const int lx16 = (lane ^ (kq << 3)) << 4;
        const int so3 = WS_WT + (T_W3 + (mem * 4 + (w >> 1)) * 28) * 2048 + (w & 1) * 1024;
        const int uo = WS_U + (grp * 2 * 112 + mem * 16 + w * 2) * 1024;
        int m0 = mem + gi * 2; m0 = m0 >= 7 ? m0 - 7 : m0;
        auto ks5 = [&](int i, int v) { int m = m0 + (i >> 1); m = m >= 7 ? m - 7 : m; return m * 16 + v * 2 + (i & 1); };
        v4u hres, W4[17];
        f32x16 a0, a1;
        f32x4 e, od;
        auto r7 = [&](int i) { const int j = i + m0; return j >= 7 ? j - 7 : j; };
        auto ld3 = [&](int i) { W3[i] = ld_sc1(rws, l16, so3 + ((i / 7) * 7 + r7(i % 7)) * 2048); };
        auto so4 = [&](int i, int v) { return WS_WT + (T_W4 + mem * 112 + ks5(i >> 1, v)) * 2048 + (i & 1) * 1024; };
        auto l4 = [&](int i, int v) { return !(i & 1) ? L_COMB + v * 6144 + (i >> 1) * 1024 : i < 8 ? L_W4B + v * 4096 + (i >> 1) * 1024 : L_W4A + v * 1024; };
        auto ld4l = [&](int i, int v) { glds_sc1(rws, lx16, so4(i, v), lb + l4(i, v)); };
        auto ld4 = [&](int i) { W4[i - 11] = ld_sc1(rws, lx16, so4(i, w)); };
        auto ldw = [&](int t0w, int i) { glds_sc1(rws, l16, WS_WT + (t0w + mem * 28 + (w >> 1) * 7 + r7(i)) * 2048 + (w & 1) * 1024, lb + L_WS + ((w >> 1) * 7 + i) * 2048 + (w & 1) * 1024); };
        {
            auto ldo = [&](int i) { glds_sc1(rws, l16, WS_WT + (T_WO + mem * 32 + (w >> 1) * 8 + ((i + m0) & 7)) * 2048 + (w & 1) * 1024, lb + L_WS + ((w >> 1) * 8 + i) * 2048 + (w & 1) * 1024); };
            if (!rt) { ldo(0); ldo(1); ldo(2); ldo(3); }
            __builtin_amdgcn_s_barrier();
            if (rt) { ldo(0); ldo(1); ldo(2); ldo(3); }
            ldo(4); ldo(5); ldo(6); ldo(7);
            v4u A[8], wf[6];
            const int wb = L_WS + kq * 8 * 2048 + lx16;
            auto ao = [&](int i) { return WS_O + (RT * 32 + kq * 8 + ((i + m0) & 7)) * 1024; };
            unpack8(xres, e, od);
            PIN(e); PIN(od);
            __builtin_amdgcn_s_barrier();
            issue<8>(A, rws, l16, ao);
            VMCNT(8);
            __syncthreads();
            small_pre<6>(L, wf, wb);
            STAMP(25);
            settle<8>(A, rws, l16, ao);
            STAMP(10);
            small_mma<8, 6>(L, A, wf, wb, a0, a1, [&](int i) {
                if (i == 4) st_sc1(sent4, rws, l16, WS_X1 + own);
                if (i >= 8 && !(i & 1)) ld3((i - 8) >> 1);
            });
            small_comb(L, a0, a1, w, l16, e, od, [&]() { ld3(4); ld3(5); }, [&]() { ld3(6); });
            hres = pack8(e, od);
            st_sc1(hres, rws, l16, WS_H + own);
            SB();
        }
        STAMP(11);
        {
            __builtin_amdgcn_s_barrier();
#pragma unroll
            for (int i = 0; i < 7; i++) ldw(T_W1, i);
            v4u A[7], wf[6], C[2];
            const int wb = L_WS + kq * 7 * 2048 + lx16;
            auto ao = [&](int i) { return WS_H + (RT * 28 + kq * 7 + r7(i)) * 1024; };
            auto co = [&](int i) { return WS_CS + mem * 256 + kq * 32 + i * 128; };
            __builtin_amdgcn_s_barrier();
            issue<7>(A, rws, l16, ao);
            VMCNT(7);
            __syncthreads();
            small_pre<6>(L, wf, wb);
            STAMP(12);
            small_mma<7, 6>(L, A, wf, wb, a0, a1, [&](int i) {
                if (i == 5) C[0] = ld_sc1(rws, h * 16, co(0));
                if (i == 7) C[1] = ld_sc1(rws, h * 16, co(1));
                if (i >= 6 && !(i & 1)) ld3(7 + ((i - 6) >> 1));
            });
            if (__builtin_expect(any_nan(a1[0]), 0)) { COUNT(34); settle<7>(A, rws, l16, ao); small_redo<7>(L, A, wb, a0, a1); }
            STAMP(26);
            float a = 0.f, q = 0.f;
            f32x2 st[4];
            e = f32x4{0.f, 0.f, 0.f, 0.f}; od = e;
            small_comb(L, a0, a1, w, l16, e, od, [&]() {
#pragma unroll
                for (int i = 0; i < 7; i++) { a = dsum(A[i], a); q = dsq(A[i], q); if (i & 1) ld3(11 + (i >> 1)); SB(); }
                *(LAS f32x2*)(L + a_st) = f32x2{a, q};
                st_sc1(sent4, rws, l16, uo);
            }, [&]() {
#pragma unroll
                for (int i = 0; i < 4; i++) st[i] = *(LAS f32x2*)(L + L_ST + (rt * 4 + i) * 512 + lane * 8);
                st_sc1(sent4, rws, l16, uo + 1024);
            });
            settle<2>(C, rws, h * 16, co);
            ld4l(1, w); ld4l(3, w);
            const f32x2 sx = (st[0] + st[1]) + (st[2] + st[3]);
            const float m = half_add(sx[0]) * (1.f / D);
            const float var = fmaxf(half_add(sx[1]) * (1.f / D) - m * m, 0.f);
            const float r1 = __builtin_amdgcn_rsqf(var + EPS);
            const float sc = r1 * __builtin_amdgcn_rsqf(var * r1 * r1 + EPS);
            e = (e - m * __builtin_bit_cast(f32x4, C[0])) * sc; od = (od - m * __builtin_bit_cast(f32x4, C[1])) * sc;
            v4u t;
#pragma unroll
            for (int j = 0; j < 4; j++) t[j] = pk(gelu(e[j]), gelu(od[j]));
            st_sc1(t, rws, l16, WS_T + own);
            SB();
            STAMP(13);
            if (!rt) { ld4l(5, w); ld4l(5, w + 4); ld4l(7, w); }
            __builtin_amdgcn_s_barrier();
#pragma unroll
            for (int i = 0; i < 7; i++) ldw(T_W2, i);
            unpack8(hres, e, od);
            e = (e - m) * r1; od = (od - m) * r1;
            PIN(e); PIN(od);
        }
        {
            v4u A[7], wf[6];
            const int g = RT * 32 + s + (kq == 0 ? -1 : kq == 1 ? 1 : kq == 2 ? -2 : 2);
            const int av = (g >= 0 && g < S) ? (g >> 5) * 28 * 1024 + (h * 32 + (g & 31)) * 16 : OOB, wb = L_WS + kq * 7 * 2048 + lx16;
            auto ao = [&](int i) { return WS_T + (kq * 7 + r7(i)) * 1024; };
            __builtin_amdgcn_s_barrier();
            issue<7>(A, rws, av, ao);
            VMCNT(7);
            __syncthreads();
            small_pre<6>(L, wf, wb);
            STAMP(27);
            settle<7>(A, rws, av, ao);
            STAMP(14);
            small_mma<7, 6>(L, A, wf, wb, a0, a1, [&](int i) {
                if (i == 1) st_sc1(sent4, rws, l16, uo + 112 * 1024);
                if (i == 3) st_sc1(sent4, rws, l16, uo + 113 * 1024);
                if (i >= 6 && !(i & 1)) ld3(14 + ((i - 6) >> 1));
            });
            small_comb(L, a0, a1, w, l16, e, od, [&]() { ld3(18); ld3(19); }, [&]() { ld3(20); });
            const v4u x1 = pack8(e, od);
            st_sc1(x1, rws, l16, WS_X1 + own);
            SB();
            STAMP(15);
            __builtin_amdgcn_s_barrier();
#pragma unroll
            for (int i = 21; i < 28; i++) ld3(i);
            f32x4 he, ho;
            unpack8(x1, e, od); unpack8(hres, he, ho);
            e += he; od += ho;
            PIN(e); PIN(od);
        }
        v4u A[14];
        auto au = [&](int i) { return WS_U + (grp * 2 * 112 + ks5(i, w)) * 1024; };
        auto au1 = [&](int i) { return au(i) + 112 * 1024; };
        {
            v4u X[7];
            auto ao = [&](int i) { return WS_X1 + (RT * 28 + kq * 7 + r7(i)) * 1024; };
            unsigned c3 = __builtin_amdgcn_raw_buffer_load_b32(rws, hi ? OOB : s * 4, WS_CS + (14 + mem * 8 + w) * 128, 16);
            __builtin_amdgcn_s_barrier();
            issue<7>(X, rws, l16, ao);
            for (unsigned sp = 0; __builtin_amdgcn_ballot_w64(c3 == SENT) != 0ull && sp < SPIN_CAP; sp++) {
                asm volatile("" ::: "memory");
                c3 = __builtin_amdgcn_raw_buffer_load_b32(rws, hi ? OOB : s * 4, WS_CS + (14 + mem * 8 + w) * 128, 16);
            }
            v4u ca = corr_a(fb(c3));
            PIN(ca);
            STAMP(16);
            const int xo = L_WS + (rt * 28 + kq * 7) * 1024 + l16;
            float a = 0.f, q = 0.f;
#pragma unroll
            for (int i = 0; i < 7; i++) { lds_w128(L, xo + i * 1024, X[i]); a = dsum(X[i], a); q = dsq(X[i], q); SB(); }
            if (__builtin_expect(any_nan(a), 0)) {
                COUNT(35);
                settle<7>(X, rws, l16, ao);
                a = 0.f; q = 0.f;
#pragma unroll
                for (int i = 0; i < 7; i++) { lds_w128(L, xo + i * 1024, X[i]); a = dsum(X[i], a); q = dsq(X[i], q); }
            }
            *(LAS f32x2*)(L + a_st) = f32x2{a, q};
            __syncthreads();
            STAMP(28);
            if (rt) {
#pragma unroll
                for (int i = 0; i < 12; i += 2) { ld4l(i, w - 4); ld4l(i, w); }
                ld4l(7, w); ld4l(9, w - 4); ld4l(9, w);
            }
            __builtin_amdgcn_s_setprio(3);
            v4u af[4];
#pragma unroll
            for (int i = 0; i < 4; i++) af[i] = lds_r128(L, L_WS + i * 1024 + l16);
#pragma unroll
            for (int r2 = 0; r2 < 2; r2++) {
                float s0 = 0.f, s1 = 0.f, c1, c2, ir;
                v4u cbx;
                a0 = zero16();
#pragma unroll
                for (int i = 0; i < 28; i++) {
                    a0 = mfma32(W3[i], af[i & 3], a0);
                    if (r2 * 28 + i + 4 < 56) af[i & 3] = lds_r128(L, L_WS + (r2 * 28 + i + 4) * 1024 + l16);
                    if (i >= 1 && i < 5) { const f32x2 v = *(LAS f32x2*)(L + L_ST + (r2 * 4 + i - 1) * 512 + lane * 8); s0 += v[0]; PIN(s0); s1 += v[1]; PIN(s1); }
                    if (i == 7) { s0 = half_add(s0) * (1.f / D); PIN(s0); }
                    if (i == 9) { s1 = fmaxf(half_add(s1) * (1.f / D) - s0 * s0, 0.f) + EPS; PIN(s1); }
                    if (i == 11) { cbx = corr_b(s0); PIN(cbx); }
                    if (i == 13) { const float rs = __builtin_amdgcn_rsqf(s1); ir = s1 * rs; c1 = GK1 * rs; c2 = GK2 * rs * rs * rs; PIN(ir); PIN(c1); PIN(c2); }
                    if (i == 15) a0 = mfma32(ca, cbx, a0);
                    if (r2 == 0) { if ((i & 3) == 2) ld4(11 + (i >> 2)); } else if (i >= 4 && i < 16 && (i & 3) == 0) ld4(25 + (i >> 2) - 1);
                    SB();
                }
                if (r2 == 0) { __builtin_amdgcn_s_setprio(2); STAMP(29); }
                else {
                    __builtin_amdgcn_s_setprio(1);
                    if (rt) { VMCNT(0); __syncthreads(); }
                    STAMP(17);
                    issue<14>(A, rws, l16, au);
                }
#pragma unroll
                for (int q2 = 0; q2 < 2; q2++) {
                    v4u u;
#pragma unroll
                    for (int j = 0; j < 4; j++) u[j] = pk(gelu_s(a0[8 * q2 + 2 * j], c1, c2, ir), gelu_s(a0[8 * q2 + 2 * j + 1], c1, c2, ir));
                    st_sc1(u, rws, l16, uo + (r2 * 112 + q2) * 1024);
                }
                SB();
                if (r2 == 0) {
#pragma unroll
                    for (int i = 18; i < 25; i++) ld4(i);
                    SB();
                }
            }
        }
        {
            if (!rt) __syncthreads();
            auto w4f = [&](int i) { return i < 11 ? lds_r128(L, l4(i, w) + l16) : W4[i - 11]; };
#pragma unroll
            for (int r2 = 0; r2 < 2; r2++) {
                v4u wl[11];
#pragma unroll
                for (int i = 0; i < 4; i++) wl[i] = w4f(i);
                SB();
                a0 = zero16(); a1 = zero16();
#pragma unroll
                for (int i = 0; i < 28; i++) {
                    const v4u wv = i < 11 ? wl[i] : W4[i - 11];
                    if (i & 1) a1 = mfma32(wv, A[i >> 1], a1); else a0 = mfma32(wv, A[i >> 1], a0);
                    if (i + 4 < 11) wl[i + 4] = w4f(i + 4);
                    SB();
                }
                if (__builtin_expect(any_nan(a1[0]), 0)) {
                    COUNT(36 + r2);
                    if (r2) settle<14>(A, rws, l16, au1); else settle<14>(A, rws, l16, au);
                    a0 = zero16(); a1 = zero16();
#pragma unroll
                    for (int i = 0; i < 14; i++) { a0 = mfma32(w4f(2 * i), A[i], a0); a1 = mfma32(w4f(2 * i + 1), A[i], a1); }
                }
                if (r2 == 0) {
                    STAMP(18);
                    __builtin_amdgcn_s_setprio(0);
                    issue<14>(A, rws, l16, au1);
                } else STAMP(19);
#pragma unroll
                for (int g = 0; g < 4; g++) {
                    if (g == 0 && rt == r2) { e += f32x4{a0[0], a0[1], a0[2], a0[3]}; od += f32x4{a1[0], a1[1], a1[2], a1[3]}; continue; }
                    const int o0 = r2 ? L_COMB + w * 6144 + g * 1024 : ((g ^ kq) * 7 + rt * 4 + g - 1) * 2048;
                    const int o1 = r2 ? L_W4B + w * 4096 + g * 1024 : o0 + 1024;
                    lds_w4f(L, o0 + l16, a0, g);
                    lds_w4f(L, o1 + l16, a1, g);
                }
                SB();
            }
            __syncthreads();
#pragma unroll
            for (int j = 1; j < 8; j++) {
                const int sw = w ^ j;
                const int o0 = rt ? L_COMB + sw * 6144 + (j & 3) * 1024 : (kq * 7 + j - 1) * 2048;
                const int o1 = rt ? L_W4B + sw * 4096 + (j & 3) * 1024 : o0 + 1024;
                e += *(LAS f32x4*)(L + o0 + l16);
                od += *(LAS f32x4*)(L + o1 + l16);
            }
            *(GAS v4u*)((GAS char*)out + (RT * 32 + s) * 896 + mem * 128 + kq * 32 + h * 16) = pack8(e, od);
        }
    }
    STAMP(20);
    asm volatile(".Lhk_end:");
}
}

extern "C" void kernel_launch(void* const* d_in, const int* in_sizes, int n_in, void* d_out, int out_size, void* d_ws, size_t ws_size, hipStream_t stream) {
    using hk::u16;
    hk::fused<<<256, 512, 0, stream>>>((const u16*)d_in[0], (const u16*)d_in[1], (const u16*)d_in[2], (const u16*)d_in[3], (const u16*)d_in[5], (const u16*)d_in[6],
                                       (const u16*)d_in[7], (const u16*)d_in[8], (u16*)d_out, (unsigned char*)d_ws);
}
```

```cpp
#include <hip/hip_runtime.h>
#include <stdint.h>

namespace hk {
typedef unsigned v4u __attribute__((ext_vector_type(4)));
typedef unsigned v2u __attribute__((ext_vector_type(2)));
typedef float f32x16 __attribute__((ext_vector_type(16)));
typedef float f32x4 __attribute__((ext_vector_type(4)));
typedef float f32x2 __attribute__((ext_vector_type(2)));
typedef short s16x8 __attribute__((ext_vector_type(8)));
typedef short s16x4 __attribute__((ext_vector_type(4)));
typedef __bf16 bf16x2 __attribute__((ext_vector_type(2)));
typedef unsigned short u16;
#define GAS __attribute__((address_space(1)))
#define LAS __attribute__((address_space(3)))
#define DI __device__ __forceinline__
#define SB() __builtin_amdgcn_sched_barrier(0)
#define PIN(x) asm volatile("" : "+v"(x))
typedef __amdgpu_buffer_rsrc_t rsrc_t;

constexpr int S = 2048, D = 448, F = 1792;
constexpr float EPS = 1e-5f, LOG2E = 1.4426950408889634f;
constexpr unsigned SENT = 0xFFFFFFFFu;
constexpr unsigned ONES = 0x3F803F80u;
constexpr int OOB = 0x7FFF0000;
constexpr int WS_E = 0, WS_FLA = 2048, WS_FLP = 4096, WS_CS = 8192, WS_WT = 65536;
constexpr int BLK = 14336, G_WO = 0, G_W1 = 28, G_W2 = 56, G_W3 = 84, G_W4 = 196, G_END = 308, T_END = G_END * 7;
constexpr int WS_KVP = WS_WT + G_END * BLK;
constexpr int RTB = 28 * 1024;
constexpr int WS_O = WS_KVP + 256 * 4096;
constexpr int WS_H = WS_O + 64 * RTB;
constexpr int WS_T = WS_H + 64 * RTB;
constexpr int WS_X1 = WS_T + 64 * RTB;
constexpr int WS_U = WS_X1 + 64 * RTB;
constexpr int WS_END = WS_U + 64 * 4 * RTB;
constexpr int WS_DBG = 32 * 1024 * 1024;
constexpr int L_SLOT = 24576, L_WQ = 98304, L_IMG = 65536, L_JUNK = 159744;
constexpr int L_WPHI = 147456, L_ST = 149504, L_CS = 153600, L_KVF = 155648;
constexpr int L_WS = 0, L_COMB = 57344, L_W4B = 106496;
constexpr int IMGS = 200;
constexpr unsigned SPIN_CAP = 1u << 16;

DI f32x16 mfma32(v4u a, v4u b, f32x16 c) { return __builtin_amdgcn_mfma_f32_32x32x16_bf16(__builtin_bit_cast(s16x8, a), __builtin_bit_cast(s16x8, b), c, 0, 0, 0); }
DI f32x4 mfma16(v4u a, v4u b, f32x4 c) { return __builtin_amdgcn_mfma_f32_16x16x32_bf16(__builtin_bit_cast(s16x8, a), __builtin_bit_cast(s16x8, b), c, 0, 0, 0); }
DI unsigned pk(float a, float b) { f32x2 p = {a, b}; return __builtin_bit_cast(unsigned, __builtin_convertvector(p, bf16x2)); }
DI float blo(unsigned u) { return __builtin_bit_cast(float, u << 16); }
DI float bhi(unsigned u) { return __builtin_bit_cast(float, u & 0xFFFF0000u); }
DI float dot2(unsigned a, unsigned b, float c) { return __builtin_amdgcn_fdot2_f32_bf16(__builtin_bit_cast(bf16x2, a), __builtin_bit_cast(bf16x2, b), c, false); }
DI float dsum(v4u a, float c) { return dot2(a.w, ONES, dot2(a.z, ONES, dot2(a.y, ONES, dot2(a.x, ONES, c)))); }
DI float dsq(v4u a, float c) { return dot2(a.w, a.w, dot2(a.z, a.z, dot2(a.y, a.y, dot2(a.x, a.x, c)))); }
DI float fb(unsigned u) { return __builtin_bit_cast(float, u); }
DI unsigned ub(float f) { return __builtin_bit_cast(unsigned, f); }
DI float half_add(float v) { auto r = __builtin_amdgcn_permlane32_swap(ub(v), ub(v), false, false); return fb(r[0]) + fb(r[1]); }
DI float half_max(float v) { auto r = __builtin_amdgcn_permlane32_swap(ub(v), ub(v), false, false); return fmaxf(fb(r[0]), fb(r[1])); }
DI float half_min(float v) { auto r = __builtin_amdgcn_permlane32_swap(ub(v), ub(v), false, false); return fminf(fb(r[0]), fb(r[1])); }
DI f32x16 zero16() { f32x16 z; _Pragma("unroll") for (int i = 0; i < 16; i++) z[i] = 0.f; return z; }
DI v4u ld_sc1(rsrc_t rs, int voff, int soff = 0) { return __builtin_amdgcn_raw_buffer_load_b128(rs, voff, soff, 16); }
DI void st_sc1(v4u v, rsrc_t rs, int voff, int soff = 0) { __builtin_amdgcn_raw_buffer_store_b128(v, rs, voff + soff, 0, 16); }
template <int OFF> DI void glds(rsrc_t rs, int voff, int soff, unsigned lds) {
    asm volatile("s_add_u32 m0, %0, %4\n\ts_nop 0\n\tbuffer_load_dwordx4 %1, %2, %3 offen lds" ::"s"(lds), "v"(voff), "s"(rs), "s"(soff), "i"(OFF) : "scc", "memory");
}
template <int OFF> DI void glds_sc1(rsrc_t rs, int voff, int soff, unsigned lds) {
    asm volatile("s_add_u32 m0, %0, %4\n\ts_nop 0\n\tbuffer_load_dwordx4 %1, %2, %3 offen sc1 lds" ::"s"(lds), "v"(voff), "s"(rs), "s"(soff), "i"(OFF) : "scc", "memory");
}
#define SPIN_(x) asm volatile("" : "+s"(x))
#define SPINR(x) do { x = __builtin_amdgcn_readfirstlane(x); asm volatile("" : "+s"(x)); } while (0)
template <int I> struct IC { static constexpr int v = I; };
template <int A, int B, class F> DI void sfor(F f) { if constexpr (A < B) { f(IC<A>{}); sfor<A + 1, B>(f); } }
#define VMCNT(n) asm volatile("s_waitcnt vmcnt(" #n ")" ::: "memory")
DI v4u lds_r128(LAS char* L, int off) { return *(LAS v4u*)(L + off); }
DI void lds_w128(LAS char* L, int off, v4u v) { *(LAS v4u*)(L + off) = v; }
DI void lds_w4f(LAS char* L, int off, const f32x16& a, int g) { *(LAS f32x4*)(L + off) = f32x4{a[4 * g], a[4 * g + 1], a[4 * g + 2], a[4 * g + 3]}; }
DI v4u tr2(LAS char* L, int off0, int off1) {
    s16x4 a = __builtin_amdgcn_ds_read_tr16_b64_v4i16((LAS s16x4*)(L + off0));
    s16x4 b = __builtin_amdgcn_ds_read_tr16_b64_v4i16((LAS s16x4*)(L + off1));
    v2u x = __builtin_bit_cast(v2u, a), y = __builtin_bit_cast(v2u, b);
    return v4u{x.x, x.y, y.x, y.y};
}
DI unsigned umax3(unsigned a, unsigned b, unsigned c) { return max(max(a, b), c); }
DI bool bad1(const v4u& a) { return __builtin_amdgcn_ballot_w64(max(umax3(a.x, a.y, a.z), a.w) == SENT) != 0ull; }
DI bool any_nan(float v) { return __builtin_amdgcn_ballot_w64(v != v) != 0ull; }
template <int N, class Vo, class So> DI void issue(v4u (&A)[N], rsrc_t rs, Vo vo, So so) {
#pragma unroll
    for (int i = 0; i < N; i++) A[i] = ld_sc1(rs, vo(i), so(i));
    SB();
}
template <int N> DI bool any_sent(const v4u (&A)[N]) {
    unsigned m = 0;
#pragma unroll
    for (int i = 0; i < N; i++) { m = umax3(m, A[i].x, A[i].y); m = umax3(m, A[i].z, A[i].w); }
    return __builtin_amdgcn_ballot_w64(m == SENT) != 0ull;
}
template <int N, class Vo, class So> DI void refetch(v4u (&A)[N], rsrc_t rs, Vo vo, So so) {
    unsigned sp = 0;
    do {
#pragma unroll
        for (int i = 0; i < N; i++) {
            const unsigned m = max(umax3(A[i].x, A[i].y, A[i].z), A[i].w);
            asm volatile("v_cmp_eq_u32 vcc, -1, %1\n\ts_nop 4\n\ts_cbranch_vccz 1f\n\tbuffer_load_dwordx4 %0, %2, %3, %4 offen sc1\n1:"
                         : "+v"(A[i]) : "v"(m), "v"(vo(i)), "s"(rs), "s"(so(i)) : "vcc", "memory");
        }
        VMCNT(0);
#pragma unroll
        for (int i = 0; i < N; i++) PIN(A[i]);
    } while (any_sent<N>(A) && ++sp < SPIN_CAP);
}
DI v4u corr_a(float c) { unsigned h = pk(c, c); float lo = c - blo(h); return v4u{(h & 0xFFFFu) | (pk(lo, lo) << 16), h & 0xFFFFu, 0u, 0u}; }
DI v4u corr_b(float m) { unsigned h = pk(-m, -m); float lo = -m - blo(h); return v4u{h, pk(lo, lo) & 0xFFFFu, 0u, 0u}; }
constexpr float GK1 = -2.f * 0.7978845608028654f * LOG2E, GK2 = GK1 * 0.044715f;
DI float gelu(float x) {
    float e = __builtin_amdgcn_exp2f(x * (x * x * GK2 + GK1));
    return x * __builtin_amdgcn_rcpf(1.f + e);
}
DI float gelu_s(float a, float c1, float c2, float ir) {
    float e = __builtin_amdgcn_exp2f(a * __builtin_fmaf(a * a, c2, c1));
    float y = a * __builtin_amdgcn_rcpf(__builtin_fmaf(e, ir, ir));
    PIN(y);
    return y;
}
DI float max3f(float a, float b, float c) { float r; asm("v_max3_f32 %0, %1, %2, %3" : "=v"(r) : "v"(a), "v"(b), "v"(c)); return r; }
DI float min3f(float a, float b, float c) { float r; asm("v_min3_f32 %0, %1, %2, %3" : "=v"(r) : "v"(a), "v"(b), "v"(c)); return r; }
DI void softmax2(f32x16 p, float mul, bool hi, unsigned (&o)[16]) {
#pragma unroll
    for (int i = 12; i < 16; i++) p[i] = hi ? p[0] : p[i];
    float mx = max3f(p[12], p[13], p[14]), mn = min3f(p[12], p[13], p[14]);
#pragma unroll
    for (int i = 0; i < 12; i += 2) { mx = max3f(mx, p[i], p[i + 1]); mn = min3f(mn, p[i], p[i + 1]); }
    mx = max3f(mx, p[15], p[15]); mn = min3f(mn, p[15], p[15]);
    float bx, bn;
    {
        auto a = __builtin_amdgcn_permlane32_swap(ub(mx), ub(mx), false, false);
        auto c = __builtin_amdgcn_permlane32_swap(ub(mn), ub(mn), false, false);
        bx = -max3f(fb(a[0]), fb(a[1]), fb(a[1])) * mul; bn = min3f(fb(c[0]), fb(c[1]), fb(c[1])) * mul;
    }
    f32x2 e[8], f[8];
#pragma unroll
    for (int i = 0; i < 8; i++) {
        const f32x2 q = {p[2 * i], p[2 * i + 1]};
        const f32x2 y = __builtin_elementwise_fma(q, f32x2{mul, mul}, f32x2{bx, bx}), z = __builtin_elementwise_fma(q, f32x2{-mul, -mul}, f32x2{bn, bn});
        e[i] = f32x2{__builtin_amdgcn_exp2f(y[0]), __builtin_amdgcn_exp2f(y[1])};
        f[i] = f32x2{__builtin_amdgcn_exp2f(z[0]), __builtin_amdgcn_exp2f(z[1])};
    }
    const float keep = hi ? 0.f : 1.f;
    f32x2 se = __builtin_elementwise_fma(e[6] + e[7], f32x2{keep, keep}, (e[0] + e[1]) + (e[2] + e[3]) + (e[4] + e[5]));
    f32x2 sf = __builtin_elementwise_fma(f[6] + f[7], f32x2{keep, keep}, (f[0] + f[1]) + (f[2] + f[3]) + (f[4] + f[5]));
    const float re = __builtin_amdgcn_rcpf(half_add(se[0] + se[1])), rf = __builtin_amdgcn_rcpf(half_add(sf[0] + sf[1]));
    const float re2 = re * keep, rf2 = rf * keep;
#pragma unroll
    for (int i = 0; i < 8; i++) {
        o[i] = __builtin_bit_cast(unsigned, __builtin_convertvector(e[i] * (i < 6 ? re : re2), bf16x2));
        o[8 + i] = __builtin_bit_cast(unsigned, __builtin_convertvector(f[i] * (i < 6 ? rf : rf2), bf16x2));
    }
}
DI void pack_feat(const f32x16& a, bool hi, v4u& f0, v4u& f1) {
    f0 = v4u{pk(a[0], a[1]), pk(a[2], a[3]), pk(a[4], a[5]), pk(a[6], a[7])};
    f1 = v4u{pk(a[8], a[9]), pk(a[10], a[11]), hi ? 0u : pk(a[12], a[13]), hi ? 0u : pk(a[14], a[15])};
}

#define STAMP(i)
#define COUNT(i)

constexpr int wpos(int i) { return (i & 1) * 7168 + (i >> 1) * 1024; }
template <int PD> DI void small_pre(LAS char* L, v4u (&wf)[PD], int wb) {
#pragma unroll
    for (int i = 0; i < PD; i++) wf[i] = lds_r128(L, wb + wpos(i));
    SB();
}
template <int PD, class Side> DI void small_mma(LAS char* L, const v4u (&A)[7], v4u (&wf)[PD], int wb, f32x16& a0, f32x16& a1, Side side) {
    a0 = zero16(); a1 = zero16();
#pragma unroll
    for (int i = 0; i < 14; i++) {
        if (i & 1) a1 = mfma32(wf[i % PD], A[i >> 1], a1); else a0 = mfma32(wf[i % PD], A[i >> 1], a0);
        if (i + PD < 14) wf[i % PD] = lds_r128(L, wb + wpos(i + PD));
        side(i);
        SB();
    }
}
DI void small_redo(LAS char* L, const v4u (&A)[7], int wb, f32x16& a0, f32x16& a1) {
    a0 = zero16(); a1 = zero16();
#pragma unroll
    for (int i = 0; i < 7; i++) {
        a0 = mfma32(lds_r128(L, wb + i * 1024), A[i], a0);
        a1 = mfma32(lds_r128(L, wb + i * 1024 + 7168), A[i], a1);
    }
}
template <class Side, class Side2> DI void small_comb(LAS char* L, const f32x16& a0, const f32x16& a1, const int (&cw)[3], int cr, f32x4& e, f32x4& od, Side side, Side2 side2) {
#pragma unroll
    for (int g = 1; g < 4; g++) lds_w4f(L, cw[g - 1], a0, g);
#pragma unroll
    for (int g = 1; g < 4; g++) lds_w4f(L, cw[g - 1] + 1024, a1, g);
    e += f32x4{a0[0], a0[1], a0[2], a0[3]}; od += f32x4{a1[0], a1[1], a1[2], a1[3]};
    PIN(e); PIN(od);
    side();
    __syncthreads();
    f32x4 t[6];
#pragma unroll
    for (int g = 0; g < 6; g++) t[g] = *(LAS f32x4*)(L + cr + g * 1024);
    side2();
    SB();
    e += (t[0] + t[2]) + t[4];
    od += (t[1] + t[3]) + t[5];
}
DI void unpack8(const v4u& v, f32x4& e, f32x4& od) {
    e = f32x4{blo(v.x), blo(v.y), blo(v.z), blo(v.w)};
    od = f32x4{bhi(v.x), bhi(v.y), bhi(v.z), bhi(v.w)};
}
DI v4u pack8(const f32x4& e, const f32x4& od) { return v4u{pk(e[0], od[0]), pk(e[1], od[1]), pk(e[2], od[2]), pk(e[3], od[3])}; }

__global__ void __launch_bounds__(512, 2) fused(const u16* __restrict__ x, const u16* __restrict__ wqkv, const u16* __restrict__ wphi, const u16* __restrict__ wo,
                                                const u16* __restrict__ w1, const u16* __restrict__ w2, const u16* __restrict__ w3, const u16* __restrict__ w4,
                                                u16* __restrict__ out, unsigned char* __restrict__ ws) {
    __shared__ __attribute__((aligned(16))) char smem[163840];
    LAS char* L = (LAS char*)smem;
    const unsigned lb = (unsigned)(size_t)L;
    STAMP(0);
    const int tid = threadIdx.x, lane = tid & 63;
    int w = __builtin_amdgcn_readfirstlane(tid >> 6);
    asm volatile("s_nop 4" : "+s"(w));
    const int b = blockIdx.x, xcd = b & 7, ii = b >> 3;
    const int l16 = lane * 16;
    unsigned lw1 = lb + w * 1024;
    SPIN_(lw1);
    {
        unsigned long long pc; unsigned len;
        asm volatile("s_getpc_b64 %0\n.Lhk_pc:\n\ts_mov_b32 %1, .Lhk_end-.Lhk_pc" : "=s"(pc), "=s"(len));
        const rsrc_t rc = __builtin_amdgcn_make_buffer_rsrc((void*)pc, 0, len, 0x00020000);
        if (w < 2) glds<L_JUNK>(rc, l16 + (ii * 2 + w) * 1024, 0, lw1);
    }
    SB();
    const int rbi = ii >> 3, rb = (xcd >> 1) * 4 + rbi, head = (xcd & 1) * 8 + (ii & 7);
    const rsrc_t rx = __builtin_amdgcn_make_buffer_rsrc((void*)x, 0, S * D * 2, 0x00020000);
    const rsrc_t rwq = __builtin_amdgcn_make_buffer_rsrc((void*)wqkv, 0, D * 3 * D * 2, 0x00020000);
    int rot = (ii & 7) + 2 * rbi; rot = rot >= 7 ? rot - 7 : rot;
    const int win = 56 * head - 8 * (head & 1);
    const bool strad = (0x2A54 >> head) & 1;
    const int vw = (w * 8 + (lane >> 3)) * 2688 + (1 + (((lane >> 2) ^ (lane >> 4)) & 1)) * 896 + win + (lane & 3) * 16;
    const int vx0 = (rb * 128 + w * 16 + (lane >> 3)) * 896 + (((lane & 7) ^ (lane >> 4)) << 4), vx1 = (vx0 ^ 64) + 8 * 896;
    unsigned lw2 = lb + w * 2048;
    int cx = rot * 128, cw_ = rot * 64 * 2688;
    SPIN_(lw2); SPIN_(cx); SPIN_(cw_);
    auto load_piece = [&](auto C, int k) {
        constexpr int o = (decltype(C)::v & 3) * L_SLOT;
        if (k == 0) glds<o + 16384>(rwq, vw, cw_, lw1);
        if (k == 1) glds<o>(rx, vx0, cx, lw2);
        if (k == 2) {
            glds<o + 1024>(rx, vx1, cx, lw2);
            const bool wrap = cx == 6 * 128;
            cx = wrap ? 0 : cx + 128; cw_ = wrap ? 0 : cw_ + 64 * 2688;
            SPIN_(cx); SPIN_(cw_);
        }
    };
    load_piece(IC<0>{}, 0);
    if (!strad) { glds<0>(rx, vx0, cx, lw2); glds<1024>(rx, vx1, cx, lw2); }
    __builtin_amdgcn_s_barrier();
    STAMP(21);
    const rsrc_t rws = __builtin_amdgcn_make_buffer_rsrc(ws, 0, WS_END, 0x00020000);
    v2u ev;
    asm volatile("buffer_load_dwordx2 %0, off, %1, 0 sc1" : "=v"(ev) : "s"(rws) : "memory");
    if (w >= 6) {
        const rsrc_t rph = __builtin_amdgcn_make_buffer_rsrc((void*)(wphi + head * 784), 0, 1568, 0x00020000);
        glds<L_WPHI - 6144>(rph, l16 + (w - 6) * 1024, 0, lw1);
    }
    const bool sact = ii < 28;
    const int gi = (ii * 37) >> 8, grp = xcd * 4 + gi, mem = ii - gi * 7;
    const int s = lane & 31, h = lane >> 5;
    const bool hi = h != 0;
    const v4u sent4 = {SENT, SENT, SENT, SENT};
    const int prt = w & 3, role = w >> 2;
    const int kq = w & 3, rt = w >> 2, RT = grp * 2 + rt;
    const int ctask = w * 32 + xcd * 4 + mem;
    int own = sact ? (RT * 28 + mem * 4 + kq) * 1024 + l16 : OOB;
    PIN(own);
    st_sc1(sent4, rws, !hi ? WS_KVP + (head * 16 + rb) * 4096 + w * 512 + l16 : (!sact && ctask < 70 && s < 8) ? WS_CS + ctask * 128 + s * 16 : OOB);
    st_sc1(sent4, rws, own, WS_O);
    st_sc1(sent4, rws, own, WS_H);
    st_sc1(sent4, rws, own, WS_T);
    SB();
    const int tau0 = (rb * 10 + __builtin_popcount(~0x2A54u & ((1u << head) - 1u))) * 8 + w;
    unsigned P[2][8];
    int pvo[2], pja[2], pjb[2], pso[2];
    unsigned long long pbase[2];
#pragma unroll
    for (int t = 0; t < 2; t++) {
        const int tau = min(tau0 + t * 1280, T_END - 1);
        const int g7 = (tau * 9363) >> 16, i = tau - g7 * 7, g = g7 - G_W4;
        const bool isO = g7 < G_W1, is4 = g7 >= G_W4, is3 = g7 >= G_W3 && !is4;
        pbase[t] = (unsigned long long)(t == 0 ? (isO ? wo : g7 < G_W2 ? w1 : g7 < G_W3 ? w2 : w3) : (is4 ? w4 : w3));
        const int ld = is3 ? 3584 : 896;
        const int col = is4 ? g >> 4 : (g7 - (isO ? G_WO : g7 < G_W2 ? G_W1 : g7 < G_W3 ? G_W2 : G_W3)) >> 2;
        const int r0 = is4 ? 256 * i + 64 * ((g >> 2) & 3) + 32 * (g & 1) + ((g >> 1) & 1)
                           : 112 * (g7 & 3) + (!isO ? 16 * i : i < 4 ? 28 * i : i < 6 ? 56 * i - 208 : 20);
        const int hs = !isO ? 8 : i < 4 ? 4 : i < 6 ? 28 : 56;
        pja[t] = (is4 ? 2 : 1) * ld; pjb[t] = (is4 ? 16 : !isO ? 4 : i < 6 ? 8 : 28) * ld;
        pvo[t] = (h * hs + r0) * ld + s * 4 + col * 128;
        pso[t] = tau0 + t * 1280 < T_END ? WS_WT + g7 * BLK + i * 1024 : OOB;
        PIN(pvo[t]); SPINR(pja[t]); SPINR(pjb[t]); SPINR(pso[t]); SPIN_(pbase[t]);
    }
    auto p_load = [&](int t) {
        const rsrc_t rw = __builtin_amdgcn_make_buffer_rsrc((void*)pbase[t], 0, D * F * 2, 0x00020000);
#pragma unroll
        for (int j = 0; j < 8; j++)
            asm volatile("buffer_load_dword %0, %1, %2, %3 offen" : "=v"(P[t][j]) : "v"(pvo[t]), "s"(rw), "s"((j & 3) * pja[t] + (j >> 2) * pjb[t]) : "memory");
    };
    auto p_store = [&](int t) {
        asm volatile("" : "+v"(P[t][0]), "+v"(P[t][1]), "+v"(P[t][2]), "+v"(P[t][3]), "+v"(P[t][4]), "+v"(P[t][5]), "+v"(P[t][6]), "+v"(P[t][7]));
        v4u e, o;
#pragma unroll
        for (int j = 0; j < 4; j++) {
            e[j] = __builtin_amdgcn_perm(P[t][2 * j + 1], P[t][2 * j], 0x05040100u);
            o[j] = __builtin_amdgcn_perm(P[t][2 * j + 1], P[t][2 * j], 0x07060302u);
        }
        st_sc1(e, rws, l16, pso[t]);
        st_sc1(o, rws, l16, pso[t] + 7168);
    };
    const int q4 = (lane & 15) >> 2, p4 = lane & 3, blk = (lane >> 4) & 1, g4 = lane >> 4;
    int ax[4];
#pragma unroll
    for (int i = 0; i < 4; i++) { ax[i] = ((prt * 32 + s) * 128 + ((h ^ ((s >> 1) & 7)) << 4)) ^ (role * 64) ^ (i * 32); PIN(ax[i]); }
    const int aw = 16384 + (8 * h + q4) * 128 + (((q4 >> 1) ^ role) & 1) * 64 + 8 * (head & 1) + blk * 32 + p4 * 8;
    int awa = aw + role * 4096, awb = aw + (role ^ 1) * 4096, awc = aw + prt * 2048;
    int a_st = L_ST + w * 512 + lane * 8, a_so = L_ST + (w ^ 4) * 512 + lane * 8, a_cs = L_CS + w * 256 + lane * 4, a_cr = L_CS + role * 1024 + lane * 4;
    int ia = L_IMG + (prt * 32 + s) * IMGS + h * 8, a_fa = L_IMG + (8 * g4 + q4) * IMGS + p4 * 8 + (w >> 1) * 32, a_fv = L_IMG + (8 * g4 + q4) * IMGS + p4 * 8 + 128 + (w & 1) * 32;
    int kvo = WS_KVP + (head * 16 + rb) * 4096 + w * 512 + ((g4 >> 1) * 16 + (lane & 15)) * 16 + (g4 & 1) * 8;
    int vq = (prt * 16 + (lane >> 2)) * 2688 + win + (lane & 3) * 16;
    PIN(awa); PIN(awb); PIN(awc); PIN(a_st); PIN(a_so); PIN(a_cs); PIN(a_cr); PIN(ia); PIN(a_fa); PIN(a_fv); PIN(kvo); PIN(vq);
    int r7[7];
    {
        int m0 = mem + gi * 2; m0 = m0 >= 7 ? m0 - 7 : m0;
#pragma unroll
        for (int i = 0; i < 7; i++) { r7[i] = (i + m0 >= 7 ? i + m0 - 7 : i + m0) * 1024; SPIN_(r7[i]); }
    }
    unsigned long long outp = (unsigned long long)out;
    SPIN_(outp);
    SB();
    if (strad) { glds<0>(rx, vx0, cx, lw2); glds<1024>(rx, vx1, cx, lw2); }
    { const bool wrap = cx == 6 * 128; cx = wrap ? 0 : cx + 128; cw_ = wrap ? 0 : cw_ + 64 * 2688; SPIN_(cx); SPIN_(cw_); }
    load_piece(IC<1>{}, 0); load_piece(IC<1>{}, 1); load_piece(IC<1>{}, 2);
    STAMP(1);
    f32x16 acc = zero16();
    v4u xq[28];
    v4u xres, wk[2][4], wc[2], wp0, wp1;
    float sm = 0.f, sq = 0.f, cs = 0.f;
    unsigned t0 = 0, t1 = 0;
    auto stats = [&](int c) {
        sm = dsum(xq[4 * c + 1], dsum(xq[4 * c], sm));
        sq = dsq(xq[4 * c + 1], dsq(xq[4 * c], sq));
        cs = dsum(wc[c & 1], cs);
        PIN(sm); PIN(sq); PIN(cs);
    };
    sfor<0, 7>([&](auto C) {
        constexpr int c = decltype(C)::v;
        if (strad) { if (c == 2 || c == 3) VMCNT(4); else if (c == 6) VMCNT(0); else VMCNT(3); }
        else { if (c == 0) VMCNT(8); else if (c == 1) VMCNT(11); else if (c == 2) VMCNT(20); else if (c == 3) VMCNT(12); else if (c == 4) VMCNT(5); else if (c == 5) VMCNT(7); else VMCNT(2); }
        __syncthreads();
        constexpr int sb = (c & 3) * L_SLOT;
        xq[4 * c] = lds_r128(L, sb + ax[0]); xq[4 * c + 1] = lds_r128(L, sb + ax[1]);
        wc[c & 1] = tr2(L, sb + awc, sb + awc + 512);
        SB();
#pragma unroll
        for (int i = 0; i < 4; i++) {
            const int a = sb + (i < 2 ? awa : awb) + (i & 1) * 2048;
            wk[c & 1][i] = tr2(L, a, a + 512);
            if (i >= 2) xq[4 * c + i] = lds_r128(L, sb + ax[i]);
            SB();
        }
#pragma unroll
        for (int i = 0; i < 4; i++) {
            if (c < 5 && i < 3) load_piece(IC<c + 2>{}, i);
            if (c > 0) acc = mfma32(wk[(c - 1) & 1][i], xq[4 * (c - 1) + i], acc);
            SB();
        }
        if (c > 0) stats(c - 1);
        if (c == 1) {
            PIN(ev);
            const unsigned long long tag = (((unsigned long long)ev.y << 32) | ev.x) + 1ull;
            t0 = (unsigned)tag; t1 = (unsigned)(tag >> 32);
            __builtin_amdgcn_raw_buffer_store_b64(v2u{t0, t1}, rws, tid == 0 ? WS_FLA + b * 8 : (tid == 1 && strad) ? WS_FLP + b * 8 : OOB, 0, 16);
        }
        if (!strad) {
            if (c < 2) p_load(c);
            if (c == 3 || c == 4) p_store(c - 3);
        }
        if (c == 3) {
            const int a = L_WPHI + (4 * h + q4) * 56 + blk * 32 + p4 * 8;
            wp0 = tr2(L, a, a + 8 * 56);
            wp1 = tr2(L, a + 16 * 56, a + 24 * 56);
            if (hi) { wp1.z = 0; wp1.w = 0; }
            PIN(wp0); PIN(wp1);
        }
        if (c == 0) STAMP(2);
        if (c == 2) STAMP(22);
        if (c == 4) STAMP(23);
    });
    auto ldq = [&](auto N) { constexpr int n = decltype(N)::v; glds<L_WQ - 4096 + n * 4096>(rwq, vq, n * 64 * 2688, lw1); };
    stats(6);
    *(LAS f32x2*)(L + a_st) = f32x2{sm, sq};
    *(LAS float*)(L + a_cs) = cs;
    SB();
#pragma unroll
    for (int i = 0; i < 4; i++) acc = mfma32(wk[0][i], xq[24 + i], acc);
    STAMP(3);
    VMCNT(0);
    __syncthreads();
    float rstd;
    v4u cb, f0, f1;
    {
        const f32x2 so = *(LAS f32x2*)(L + a_so);
        const float c = (*(LAS float*)(L + a_cr) + *(LAS float*)(L + a_cr + 256)) + (*(LAS float*)(L + a_cr + 512) + *(LAS float*)(L + a_cr + 768));
        if (role) {
            __builtin_amdgcn_raw_buffer_store_b64(v2u{t0, t1}, rws, (tid == 256 && !strad) ? WS_FLP + b * 8 : OOB, 0, 16);
            ldq(IC<0>{}); ldq(IC<1>{});
        }
        const float mean = half_add(sm + so[0]) * (1.f / D);
        cb = corr_b(mean);
        acc = mfma32(corr_a(c), cb, acc);
        rstd = __builtin_amdgcn_rsqf(fmaxf(half_add(sq + so[1]) * (1.f / D) - mean * mean, 0.f) + EPS);
    }
    auto flags_ok = [&]() {
        const bool ok = f0.x == t0 && f0.y == t1 && f0.z == t0 && f0.w == t1 && f1.x == t0 && f1.y == t1 && f1.z == t0 && f1.w == t1;
        return __builtin_amdgcn_ballot_w64(!ok) == 0ull;
    };
    auto flags_wait = [&](int base) {
        if (__builtin_expect(!flags_ok(), 0)) {
            unsigned sp = 0;
            do {
                asm volatile("" ::: "memory");
                f0 = ld_sc1(rws, lane * 32, base); f1 = ld_sc1(rws, lane * 32 + 16, base);
                COUNT(32);
            } while (!flags_ok() && ++sp < SPIN_CAP);
        }
    };
    if (role == 0) {
        v4u k0, k1;
        pack_feat(acc, hi, k0, k1);
        unsigned ph[16];
        softmax2(mfma32(wp1, k1, mfma32(wp0, k0, zero16())), rstd * LOG2E, hi, ph);
#pragma unroll
        for (int g = 0; g < 8; g++) *(LAS v2u*)(L + ia + g * 16) = v2u{ph[2 * g], ph[2 * g + 1]};
        STAMP(24);
    } else {
#pragma unroll
        for (int g = 0; g < 4; g++) {
            v2u v = {pk(acc[4 * g] * rstd, acc[4 * g + 1] * rstd), pk(acc[4 * g + 2] * rstd, acc[4 * g + 3] * rstd)};
            if (g == 3 && hi) v = v2u{0x00003F80u, 0u};
            *(LAS v2u*)(L + ia + 128 + g * 16) = v;
        }
        SB();
        ldq(IC<2>{}); ldq(IC<3>{});
        if (!strad) { ldq(IC<4>{}); ldq(IC<5>{}); ldq(IC<6>{}); }
        STAMP(24);
    }
    __syncthreads();
    STAMP(4);
    {
        v4u fa[4], fv[4];
#pragma unroll
        for (int ks = 0; ks < 4; ks++) {
            fa[ks] = tr2(L, a_fa + ks * 32 * IMGS, a_fa + ks * 32 * IMGS + 4 * IMGS);
            fv[ks] = tr2(L, a_fv + ks * 32 * IMGS, a_fv + ks * 32 * IMGS + 4 * IMGS);
            SB();
        }
        f32x4 kv = {0.f, 0.f, 0.f, 0.f};
#pragma unroll
        for (int ks = 0; ks < 4; ks++) kv = mfma16(fa[ks], fv[ks], kv);
        __builtin_amdgcn_raw_buffer_store_b64(v2u{pk(kv[0], kv[1]), pk(kv[2], kv[3])}, rws, kvo, 0, 16);
        SB();
    }
    STAMP(5);
    const int aq = L_WQ + (8 * h + q4) * 64 + 8 * (head & 1) + blk * 32 + p4 * 8;
    const int xo = sact ? (RT * 32 + s) * 896 + mem * 128 + kq * 32 + h * 16 : OOB;
    if (role == 0) {
        xres = __builtin_amdgcn_raw_buffer_load_b128(rx, xo, 0, 0);
        if (w == 0) {
            f0 = ld_sc1(rws, lane * 32, WS_FLA); f1 = ld_sc1(rws, lane * 32 + 16, WS_FLA);
            flags_wait(WS_FLA);
            if (b == 0 && lane == 0) __builtin_amdgcn_raw_buffer_store_b64(v2u{t0, t1}, rws, WS_E, 0, 16);
        }
        v4u sel = {0u, 0u, 0u, 0u};
        {
            const int j = (lane & 15) - 8 * (g4 & 1);
            const unsigned one = (j & 1) ? 0x3F800000u : 0x00003F80u;
#pragma unroll
            for (int q = 0; q < 4; q++) sel[q] = (j >> 1) == q ? one : 0u;
        }
        int ko = L_KVF + prt * 1024 + ((g4 & 1) * 32 + (lane & 15)) * 16 + (g4 >> 1) * 8;
        int gv = WS_KVP + head * 65536 + prt * 1024 + h * 4096 + s * 16, aq7 = aq + prt * 7168;
        PIN(ko); PIN(sel); PIN(gv); PIN(aq7);
        __syncthreads();
        STAMP(6);
        v4u G[16];
        auto gvo = [&](int i) { return gv + (i & 1) * 512; };
        auto gso = [&](int i) { return (((i >> 1) + 2 * rbi) & 7) * 8192; };
        {
            v4u wf[7];
#pragma unroll
            for (int i = 0; i < 7; i++) wf[i] = tr2(L, aq7 + i * 1024, aq7 + i * 1024 + 256);
#pragma unroll
            for (int i = 0; i < 4; i++) G[i] = ld_sc1(rws, gvo(i), gso(i));
            SB();
            float cq = 0.f;
#pragma unroll
            for (int i = 0; i < 7; i++) cq = dsum(wf[i], cq);
            *(LAS float*)(L + a_cs) = cq;
            __syncthreads();
#pragma unroll
            for (int i = 4; i < 16; i++) G[i] = ld_sc1(rws, gvo(i), gso(i));
            f0 = ld_sc1(rws, w == 0 ? lane * 32 : OOB, WS_FLP); f1 = ld_sc1(rws, w == 0 ? lane * 32 + 16 : OOB, WS_FLP);
            SB();
        }
        f32x4 k0 = {0.f, 0.f, 0.f, 0.f}, k1 = k0;
#pragma unroll
        for (int i = 0; i < 8; i++) { k0 = mfma16(sel, G[2 * i], k0); k1 = mfma16(sel, G[2 * i + 1], k1); }
        STAMP(7);
        if (__builtin_expect(any_nan(k0[0] + k1[0]), 0)) {
            COUNT(33);
            refetch<16>(G, rws, gvo, gso);
            k0 = f32x4{0.f, 0.f, 0.f, 0.f}; k1 = k0;
#pragma unroll
            for (int i = 0; i < 8; i++) { k0 = mfma16(sel, G[2 * i], k0); k1 = mfma16(sel, G[2 * i + 1], k1); }
        }
        *(LAS v2u*)(L + ko) = v2u{pk(k0[0], k0[1]), pk(k0[2], k0[3])};
        *(LAS v2u*)(L + ko + 256) = v2u{pk(k1[0], k1[1]), pk(k1[2], k1[3])};
        PIN(xres);
        STAMP(8);
        __syncthreads();
        if (w == 0) flags_wait(WS_FLP);
    } else {
        if (strad) { ldq(IC<4>{}); ldq(IC<5>{}); ldq(IC<6>{}); }
        int aqc[7];
#pragma unroll
        for (int c = 0; c < 7; c++) { aqc[c] = aq + (c + rot >= 7 ? c + rot - 7 : c + rot) * 4096; PIN(aqc[c]); }
        VMCNT(0);
        xres = __builtin_amdgcn_raw_buffer_load_b128(rx, xo, 0, 0);
        __syncthreads();
        v4u wf[4];
        auto rdq = [&](int i) { const int a = aqc[i >> 2] + ((i & 3) ^ 2) * 1024; return tr2(L, a, a + 256); };
#pragma unroll
        for (int i = 0; i < 4; i++) wf[i] = rdq(i);
        SB();
        acc = zero16();
        float cq[4];
        v4u cqa;
#pragma unroll
        for (int i = 0; i < 28; i++) {
            acc = mfma32(wf[i & 3], xq[i], acc);
            if (i == 5) __builtin_amdgcn_s_barrier();
            if (i == 12) {
#pragma unroll
                for (int r = 0; r < 4; r++) cq[r] = *(volatile LAS float*)(L + a_cr - 1024 + r * 256);
            }
            if (i + 4 < 28) wf[i & 3] = rdq(i + 4);
            if (i == 17) { cq[0] = (cq[0] + cq[1]) + (cq[2] + cq[3]); PIN(cq[0]); }
            if (i == 19) { cqa = corr_a(cq[0]); PIN(cqa); }
            if (i == 21) acc = mfma32(cqa, cb, acc);
            SB();
        }
        STAMP(6);
        v4u q0, q1;
        pack_feat(acc, hi, q0, q1);
        unsigned ph[16];
        softmax2(mfma32(wp1, q1, mfma32(wp0, q0, zero16())), rstd * LOG2E, hi, ph);
#pragma unroll
        for (int i = 0; i < 16; i++) PIN(ph[i]);
        const int ob = WS_O + ((rb * 4 + prt) * 28 + (head >> 2) * 7) * 1024, hq = head & 3;
        int o0 = ob + hq * 1024 + l16, o1 = hi ? OOB : ob + 4096 + hq * 512 + l16, o2 = hi ? ob + 6144 + (hq >> 1) * 512 + s * 16 + (hq & 1) * 8 : OOB;
        PIN(o0); PIN(o1); PIN(o2); PIN(xres);
        STAMP(8);
        __syncthreads();
        v4u fa[4];
#pragma unroll
        for (int f = 0; f < 4; f++) fa[f] = lds_r128(L, L_KVF + f * 1024 + l16);
        f32x16 o = zero16();
#pragma unroll
        for (int f = 0; f < 4; f++) o = mfma32(fa[f], v4u{ph[4 * f], ph[4 * f + 1], ph[4 * f + 2], ph[4 * f + 3]}, o);
        auto r = __builtin_amdgcn_permlane32_swap(ub(o[12]), ub(o[12]), false, false);
        const float rd = __builtin_amdgcn_rcpf(fb(r[1]) + 1e-6f);
        st_sc1(v4u{pk(o[0] * rd, o[1] * rd), pk(o[2] * rd, o[3] * rd), pk(o[4] * rd, o[5] * rd), pk(o[6] * rd, o[7] * rd)}, rws, o0);
        const v4u u = {pk(o[8] * rd, o[9] * rd), pk(o[10] * rd, o[11] * rd), pk(o[12] * rd, o[13] * rd), pk(o[14] * rd, o[15] * rd)};
        st_sc1(u, rws, o1);
        __builtin_amdgcn_raw_buffer_store_b64(v2u{u.x, u.y}, rws, o2, 0, 16);
        SB();
    }
    STAMP(9);
    __syncthreads();
    v4u W3[28];
    if (!sact) {
        if (ctask < 70) {
            const int vo = WS_WT + ((ctask < 14 ? G_W1 : G_W3 - 28) + (ctask >> 1) * 4) * BLK + (ctask & 1) * 7168 + l16;
            issue<28>(W3, rws, [&](int i) { return vo; }, [&](int i) { return (i / 7) * BLK + (i % 7) * 1024; });
            float c = 0.f;
#pragma unroll
            for (int i = 0; i < 28; i++) c = dsum(W3[i], c);
            __builtin_amdgcn_raw_buffer_store_b32(ub(half_add(c)), rws, hi ? OOB : WS_CS + ctask * 128 + s * 4, 0, 16);
        }
        const int id = mem * 8 + w;
        for (int i = id; i < 784; i += 32) glds_sc1<0>(rws, l16, WS_WT + G_W1 * BLK + i * 1024, lw1);
        for (int i = id; i < 1568; i += 32) glds_sc1<0>(rws, l16, WS_WT + G_W4 * BLK + i * 1024, lw1);
        VMCNT(0);
    } else {
        unsigned lw7 = lb + w * 7168;
        SPIN_(lw7);
        int vsw = WS_WT + mem * 4 * BLK + w * 7168 + l16;
        auto ldw = [&](int g0) { const int v = vsw + g0 * BLK; sfor<0, 7>([&](auto I) { glds_sc1<L_WS + decltype(I)::v * 1024>(rws, v, r7[decltype(I)::v], lw7); }); };
        ldw(G_WO);
        const int lx16 = (lane ^ (kq << 3)) << 4;
        int v3 = WS_WT + (G_W3 + (mem * 4 + (w >> 1)) * 4) * BLK + (w & 1) * 7168 + l16;
        int v4 = WS_WT + (G_W4 + mem * 16 + w * 2) * BLK + lx16;
        int uo = WS_U + grp * 8 * RTB + w * BLK + mem * 1024 + l16;
        int av = (RT * 28 + kq * 7) * 1024 + l16, wb = L_WS + kq * BLK + lx16;
        int cw[3], cr = L_COMB + w * 6144 + l16;
#pragma unroll
        for (int g = 1; g < 4; g++) { cw[g - 1] = L_COMB + ((rt * 4 + (g ^ kq)) * 3 + (g - 1)) * 2048 + l16; PIN(cw[g - 1]); }
        unsigned lw6 = lb + L_COMB + w * 6144, lw5 = lb + L_W4B + w * 5120;
        PIN(v3); PIN(v4); PIN(uo); PIN(av); PIN(wb); PIN(cr); SPIN_(lw6); SPIN_(lw5);
        auto s7 = [&](int i) { return r7[i]; };
        v4u hres, W4[17];
        f32x16 a0, a1;
        f32x4 e, od;
        auto ld3 = [&](int i) { W3[i] = ld_sc1(rws, v3 + (i / 7) * BLK, r7[i % 7]); };
        auto ld4l = [&](auto I, auto DV) {
            constexpr int i = decltype(I)::v, dv = decltype(DV)::v;
            if (i & 1) glds_sc1<dv * 4 * 5120 + (i >> 1) * 1024>(rws, v4 + dv * 8 * BLK + (i & 3) * 7168, r7[i >> 2], lw5);
            else glds_sc1<dv * 4 * 6144 + (i >> 1) * 1024>(rws, v4 + dv * 8 * BLK + (i & 3) * 7168, r7[i >> 2], lw6);
        };
        auto ld4 = [&](int i) { W4[i - 11] = ld_sc1(rws, v4 + (i & 3) * 7168, r7[i >> 2]); };
        auto stage_in = [&](v4u (&A)[7], int vo) {
            __builtin_amdgcn_s_barrier();
            if (rt) { VMCNT(0); __builtin_amdgcn_s_barrier(); }
            issue<7>(A, rws, [&](int) { return vo; }, s7);
            if (!rt) { VMCNT(7); __builtin_amdgcn_s_barrier(); }
            asm volatile("" ::: "memory");
        };
        {
            v4u A[7], wf[6];
            unpack8(xres, e, od);
            PIN(e); PIN(od);
            const int vo = av + WS_O;
            stage_in(A, vo);
            small_pre<6>(L, wf, wb);
            STAMP(25);
            if (any_sent<7>(A)) refetch<7>(A, rws, [&](int) { return vo; }, s7);
            SB();
            STAMP(10);
            small_mma<6>(L, A, wf, wb, a0, a1, [&](int i) {
                if (i == 3) st_sc1(sent4, rws, own, WS_X1);
                if (i >= 6 && !(i & 1)) ld3((i - 6) >> 1);
            });
            small_comb(L, a0, a1, cw, cr, e, od, [&]() { ld3(4); ld3(5); }, [&]() { ld3(6); });
            hres = pack8(e, od);
            st_sc1(hres, rws, own, WS_H);
            SB();
        }
        STAMP(11);
        {
            __builtin_amdgcn_s_barrier();
            ldw(G_W1);
            v4u A[7], wf[6], C[2];
            const int vo = av + WS_H, cv = WS_CS + mem * 256 + kq * 32 + h * 16;
            int sr = L_ST + rt * 2048 + lane * 8;
            PIN(sr);
            stage_in(A, vo);
            small_pre<6>(L, wf, wb);
            STAMP(12);
            small_mma<6>(L, A, wf, wb, a0, a1, [&](int i) {
                if (i == 5) C[0] = ld_sc1(rws, cv);
                if (i == 7) C[1] = ld_sc1(rws, cv + 128);
                if (i >= 6 && !(i & 1)) ld3(7 + ((i - 6) >> 1));
            });
            if (__builtin_expect(any_nan(a1[0]), 0)) { COUNT(34); refetch<7>(A, rws, [&](int) { return vo; }, s7); small_redo(L, A, wb, a0, a1); }
            STAMP(26);
            float a = 0.f, q = 0.f;
            f32x2 st[4];
            e = f32x4{0.f, 0.f, 0.f, 0.f}; od = e;
            small_comb(L, a0, a1, cw, cr, e, od, [&]() {
#pragma unroll
                for (int i = 0; i < 7; i++) { a = dsum(A[i], a); q = dsq(A[i], q); if (i & 1) ld3(11 + (i >> 1)); SB(); }
                *(LAS f32x2*)(L + a_st) = f32x2{a, q};
                st_sc1(sent4, rws, uo);
            }, [&]() {
#pragma unroll
                for (int i = 0; i < 4; i++) st[i] = *(LAS f32x2*)(L + sr + i * 512);
                st_sc1(sent4, rws, uo + 7168);
            });
            if (__builtin_expect(any_sent<2>(C), 0)) refetch<2>(C, rws, [&](int i) { return cv + i * 128; }, [&](int) { return 0; });
            const f32x2 sx = (st[0] + st[1]) + (st[2] + st[3]);
            const float m = half_add(sx[0]) * (1.f / D);
            const float var = fmaxf(half_add(sx[1]) * (1.f / D) - m * m, 0.f);
            const float r1 = __builtin_amdgcn_rsqf(var + EPS);
            const float sc = r1 * __builtin_amdgcn_rsqf(var * r1 * r1 + EPS);
            e = (e - m * __builtin_bit_cast(f32x4, C[0])) * sc; od = (od - m * __builtin_bit_cast(f32x4, C[1])) * sc;
            v4u t;
#pragma unroll
            for (int j = 0; j < 4; j++) t[j] = pk(gelu(e[j]), gelu(od[j]));
            st_sc1(t, rws, own, WS_T);
            SB();
            STAMP(13);
            if (!rt) { ld4l(IC<1>{}, IC<0>{}); ld4l(IC<1>{}, IC<1>{}); ld4l(IC<3>{}, IC<0>{}); ld4l(IC<3>{}, IC<1>{}); }
            __builtin_amdgcn_s_barrier();
            ldw(G_W2);
            unpack8(hres, e, od);
            e = (e - m) * r1; od = (od - m) * r1;
            PIN(e); PIN(od);
        }
        {
            v4u A[7], wf[6];
            const int g = RT * 32 + s + (kq == 0 ? -1 : kq == 1 ? 1 : kq == 2 ? -2 : 2);
            const int vo = (g >= 0 && g < S) ? WS_T + (g >> 5) * RTB + kq * 7168 + (h * 32 + (g & 31)) * 16 : OOB;
            stage_in(A, vo);
            small_pre<6>(L, wf, wb);
            STAMP(27);
            if (any_sent<7>(A)) refetch<7>(A, rws, [&](int) { return vo; }, s7);
            SB();
            STAMP(14);
            small_mma<6>(L, A, wf, wb, a0, a1, [&](int i) {
                if (i == 1) st_sc1(sent4, rws, uo + 4 * RTB);
                if (i == 3) st_sc1(sent4, rws, uo + 4 * RTB + 7168);
                if (i >= 6 && !(i & 1)) ld3(14 + ((i - 6) >> 1));
            });
            small_comb(L, a0, a1, cw, cr, e, od, [&]() { ld3(18); ld3(19); }, [&]() { ld3(20); });
            const v4u x1 = pack8(e, od);
            st_sc1(x1, rws, own, WS_X1);
            SB();
            STAMP(15);
            __builtin_amdgcn_s_barrier();
#pragma unroll
            for (int i = 21; i < 28; i++) ld3(i);
            f32x4 he, ho;
            unpack8(x1, e, od); unpack8(hres, he, ho);
            e += he; od += ho;
            PIN(e); PIN(od);
        }
        v4u A[14];
        const int ub0 = uo - mem * 1024;
        auto s14 = [&](int i) { return r7[i >> 1]; };
        {
            v4u X[7];
            const int vo = av + WS_X1, cv = hi ? OOB : WS_CS + (14 + mem * 8 + w) * 128 + s * 4;
            unsigned c3 = __builtin_amdgcn_raw_buffer_load_b32(rws, cv, 0, 16);
            int xw = L_WS + w * 7168 + l16, sr = L_ST + lane * 8;
            PIN(xw); PIN(sr);
            __builtin_amdgcn_s_barrier();
            issue<7>(X, rws, [&](int) { return vo; }, s7);
            if (__builtin_expect(__builtin_amdgcn_ballot_w64(c3 == SENT) != 0ull, 0)) {
                unsigned sp = 0;
                do {
                    asm volatile("" ::: "memory");
                    c3 = __builtin_amdgcn_raw_buffer_load_b32(rws, cv, 0, 16);
                } while (__builtin_amdgcn_ballot_w64(c3 == SENT) != 0ull && ++sp < SPIN_CAP);
            }
            v4u ca = corr_a(fb(c3));
            PIN(ca);
            STAMP(16);
            float a = 0.f, q = 0.f;
#pragma unroll
            for (int i = 0; i < 7; i++) { lds_w128(L, xw + i * 1024, X[i]); a = dsum(X[i], a); q = dsq(X[i], q); SB(); }
            if (__builtin_expect(any_nan(a), 0)) {
                COUNT(35);
                refetch<7>(X, rws, [&](int) { return vo; }, s7);
                a = 0.f; q = 0.f;
#pragma unroll
                for (int i = 0; i < 7; i++) { lds_w128(L, xw + i * 1024, X[i]); a = dsum(X[i], a); q = dsq(X[i], q); }
            }
            *(LAS f32x2*)(L + a_st) = f32x2{a, q};
            __syncthreads();
            STAMP(28);
            if (rt) {
                sfor<0, 6>([&](auto I) { ld4l(IC<2 * decltype(I)::v>{}, IC<-1>{}); ld4l(IC<2 * decltype(I)::v>{}, IC<0>{}); });
                sfor<2, 5>([&](auto I) { ld4l(IC<2 * decltype(I)::v + 1>{}, IC<-1>{}); ld4l(IC<2 * decltype(I)::v + 1>{}, IC<0>{}); });
            }
            __builtin_amdgcn_s_setprio(3);
            v4u af[4];
#pragma unroll
            for (int i = 0; i < 4; i++) af[i] = lds_r128(L, L_WS + i * 1024 + l16);
#pragma unroll
            for (int r2 = 0; r2 < 2; r2++) {
                float s0, s1, c1, c2, ir;
                f32x2 st[4];
                v4u cbx;
                a0 = zero16();
#pragma unroll
                for (int i = 0; i < 28; i++) {
                    a0 = mfma32(W3[i], af[i & 3], a0);
                    if (i < 2) { st[2 * i] = *(LAS f32x2*)(L + sr + (r2 * 4 + 2 * i) * 512); st[2 * i + 1] = *(LAS f32x2*)(L + sr + (r2 * 4 + 2 * i + 1) * 512); }
                    if (r2 * 28 + i + 4 < 56) af[i & 3] = lds_r128(L, L_WS + (r2 * 28 + i + 4) * 1024 + l16);
                    if (i == 6) { s0 = st[0][0] + st[1][0]; PIN(s0); s0 += st[2][0]; PIN(s0); s0 += st[3][0]; PIN(s0); }
                    if (i == 7) { s1 = st[0][1] + st[1][1]; PIN(s1); s1 += st[2][1]; PIN(s1); s1 += st[3][1]; PIN(s1); }
                    if (i == 8) { s0 = half_add(s0) * (1.f / D); PIN(s0); }
                    if (i == 9) { s1 = half_add(s1) * (1.f / D); PIN(s1); }
                    if (i == 10) { s1 = fmaxf(__builtin_fmaf(-s0, s0, s1), 0.f) + EPS; PIN(s1); }
                    if (i == 12) { cbx = corr_b(s0); PIN(cbx); }
                    if (i == 14) { c1 = __builtin_amdgcn_rsqf(s1); PIN(c1); ir = s1 * c1; PIN(ir); c2 = c1 * c1; PIN(c2); }
                    if (i == 15) { c1 *= GK1; PIN(c1); c2 *= 0.044715f; PIN(c2); c2 *= c1; PIN(c2); }
                    if (i == 16) a0 = mfma32(ca, cbx, a0);
                    SB();
                }
                if (r2 == 0) { __builtin_amdgcn_s_setprio(2); STAMP(29); }
                else {
                    __builtin_amdgcn_s_setprio(1);
                    if (rt) { VMCNT(0); __syncthreads(); }
                    STAMP(17);
                }
                v4u u;
#pragma unroll
                for (int j = 0; j < 8; j++) {
                    if (r2 == 0) { if (j < 7) { ld4(11 + 2 * j); ld4(12 + 2 * j); } }
                    else if (j < 7) { A[2 * j] = ld_sc1(rws, ub0, r7[j]); A[2 * j + 1] = ld_sc1(rws, ub0 + 7168, r7[j]); }
                    u[j & 3] = pk(gelu_s(a0[2 * j], c1, c2, ir), gelu_s(a0[2 * j + 1], c1, c2, ir));
                    if ((j & 3) == 3) st_sc1(u, rws, uo + r2 * 4 * RTB + (j >> 2) * 7168);
                    SB();
                }
                if (r2 == 1) { ld4(25); ld4(26); ld4(27); SB(); }
            }
        }
        {
            int p5 = L_W4B + w * 5120 + l16, pw[4], pr = kq * BLK + l16;
#pragma unroll
            for (int g = 0; g < 4; g++) { pw[g] = ((g ^ kq) * 7 + rt * 4 + g - 1) * 2048 + l16; PIN(pw[g]); }
            PIN(p5); PIN(pr);
            if (!rt) __syncthreads();
            auto w4f = [&](int i) { return i >= 11 ? W4[i - 11] : lds_r128(L, ((i & 1) ? p5 : cr) + (i >> 1) * 1024); };
#pragma unroll
            for (int r2 = 0; r2 < 2; r2++) {
                v4u wl[11];
#pragma unroll
                for (int i = 0; i < 4; i++) wl[i] = w4f(i);
                SB();
                a0 = zero16(); a1 = zero16();
#pragma unroll
                for (int i = 0; i < 28; i++) {
                    const v4u wv = i < 11 ? wl[i] : W4[i - 11];
                    if (i & 1) a1 = mfma32(wv, A[i >> 1], a1); else a0 = mfma32(wv, A[i >> 1], a0);
                    if (i + 4 < 11) wl[i + 4] = w4f(i + 4);
                    SB();
                }
                auto uv = [&](int i) { return ub0 + r2 * 4 * RTB + (i & 1) * 7168; };
                if (__builtin_expect(any_nan(a1[0]), 0)) {
                    COUNT(36 + r2);
                    refetch<14>(A, rws, uv, s14);
                    a0 = zero16(); a1 = zero16();
#pragma unroll
                    for (int i = 0; i < 14; i++) { a0 = mfma32(w4f(2 * i), A[i], a0); a1 = mfma32(w4f(2 * i + 1), A[i], a1); }
                }
                if (r2 == 0) {
                    STAMP(18);
                    __builtin_amdgcn_s_setprio(0);
#pragma unroll
                    for (int i = 0; i < 14; i++) {
                        A[i] = ld_sc1(rws, uv(i) + 4 * RTB, s14(i));
                        if (i >= 2 && i < 8) lds_w4f(L, pw[i >> 1] + (i & 1) * 1024, (i & 1) ? a1 : a0, i >> 1);
                        SB();
                    }
                    if (rt) { lds_w4f(L, pw[0], a0, 0); lds_w4f(L, pw[0] + 1024, a1, 0); __syncthreads(); }
                } else {
                    STAMP(19);
#pragma unroll
                    for (int g = 1; g < 4; g++) lds_w4f(L, cr + g * 1024, a0, g);
#pragma unroll
                    for (int g = 1; g < 4; g++) lds_w4f(L, p5 + g * 1024, a1, g);
                    if (!rt) { lds_w4f(L, cr, a0, 0); lds_w4f(L, p5, a1, 0); }
                }
                if (rt == r2) { e += f32x4{a0[0], a0[1], a0[2], a0[3]}; od += f32x4{a1[0], a1[1], a1[2], a1[3]}; }
                SB();
            }
            __syncthreads();
            f32x4 t[14];
            if (!rt) {
#pragma unroll
                for (int j = 0; j < 14; j++) t[j] = *(LAS f32x4*)(L + pr + j * 1024);
            } else {
#pragma unroll
                for (int j = 1; j < 8; j++) {
                    t[2 * j - 2] = *(LAS f32x4*)(L + L_COMB + (w ^ j) * 6144 + (j & 3) * 1024 + l16);
                    t[2 * j - 1] = *(LAS f32x4*)(L + L_W4B + (w ^ j) * 5120 + (j & 3) * 1024 + l16);
                }
            }
            SB();
            e += ((t[0] + t[2]) + (t[4] + t[6])) + ((t[8] + t[10]) + t[12]);
            od += ((t[1] + t[3]) + (t[5] + t[7])) + ((t[9] + t[11]) + t[13]);
            *(GAS v4u*)((GAS char*)outp + xo) = pack8(e, od);
            if (!rt) __builtin_amdgcn_s_barrier();
        }
    }
    STAMP(20);
    asm volatile(".Lhk_end:");
}
}

extern "C" void kernel_launch(void* const* d_in, const int* in_sizes, int n_in, void* d_out, int out_size, void* d_ws, size_t ws_size, hipStream_t stream) {
    using hk::u16;
    hk::fused<<<256, 512, 0, stream>>>((const u16*)d_in[0], (const u16*)d_in[1], (const u16*)d_in[2], (const u16*)d_in[3], (const u16*)d_in[5], (const u16*)d_in[6],
                                       (const u16*)d_in[7], (const u16*)d_in[8], (u16*)d_out, (unsigned char*)d_ws);
}
```

```cpp
#include <hip/hip_runtime.h>
#include <stdint.h>

namespace hk {
typedef unsigned v4u __attribute__((ext_vector_type(4)));
typedef unsigned v2u __attribute__((ext_vector_type(2)));
typedef float f32x16 __attribute__((ext_vector_type(16)));
typedef float f32x4 __attribute__((ext_vector_type(4)));
typedef float f32x2 __attribute__((ext_vector_type(2)));
typedef short s16x8 __attribute__((ext_vector_type(8)));
typedef short s16x4 __attribute__((ext_vector_type(4)));
typedef __bf16 bf16x2 __attribute__((ext_vector_type(2)));
typedef unsigned short u16;
#define GAS __attribute__((address_space(1)))
#define LAS __attribute__((address_space(3)))
#define DI __device__ __forceinline__
#define SB() __builtin_amdgcn_sched_barrier(0)
#define PIN(x) asm volatile("" : "+v"(x))
typedef __amdgpu_buffer_rsrc_t rsrc_t;

constexpr int S = 2048, D = 448, F = 1792;
constexpr float EPS = 1e-5f, LOG2E = 1.4426950408889634f;
constexpr unsigned SENT = 0xFFFFFFFFu;
constexpr unsigned ONES = 0x3F803F80u;
constexpr int OOB = 0x7FFF0000;
constexpr int WS_E = 0, WS_F2 = 2048, WS_FA = 4096, WS_F3 = 20480, WS_CS = 8192, WS_WT = 32768;
constexpr int BLK = 14336, G_W3 = 0, G_W4 = 112, G_END = 224, T_END = G_END * 7;
constexpr int WS_KVP = WS_WT + G_END * BLK;
constexpr int RTB = 28 * 1024;
constexpr int WS_O = WS_KVP + 256 * 4096;
constexpr int WS_H = WS_O + 64 * RTB;
constexpr int WS_T = WS_H + 64 * RTB;
constexpr int WS_X1 = WS_T + 64 * RTB;
constexpr int WS_U = WS_X1 + 64 * RTB;
constexpr int WS_SS = WS_U + 64 * 4 * RTB;
constexpr int WS_END = WS_SS + 2 * 64 * 7 * 256;
constexpr int WS_DBG = 32 * 1024 * 1024;
constexpr int L_WK = 114688, L_WV = 0, L_WQ = 28672, L_IMG = 57408, L_JUNK = 159744, L_SY = 163776;
constexpr int L_WPHI = 147456, L_ST = 149504, L_CS = 153600, L_KVF = 155648;
constexpr int L_WS = 0, L_COMB = 57344, L_W4B = 106496, L_XR = 139264;
constexpr int IMGS = 200;
constexpr unsigned SPIN_CAP = 1u << 16;
#define EARLYW 3
#define GPRIO 3
#define PFY 3
#define FT 0
#define FTA0 20
#define FTB0 16
#define ASP 7
#define GSLEEP 6
#define WARM3 0
#define CHK 7
#define OUTAUX 0
#define YV 2
#define ARM0 2
#define VPRIO 1
#define EQ 0
#define XP 0
#define RECODE 1
#define NOP 0
#define OUTAUX0 0
#define SLP 1
#define NCLS 7

DI f32x16 mfma32(v4u a, v4u b, f32x16 c) { return __builtin_amdgcn_mfma_f32_32x32x16_bf16(__builtin_bit_cast(s16x8, a), __builtin_bit_cast(s16x8, b), c, 0, 0, 0); }
DI f32x4 mfma16(v4u a, v4u b, f32x4 c) { return __builtin_amdgcn_mfma_f32_16x16x32_bf16(__builtin_bit_cast(s16x8, a), __builtin_bit_cast(s16x8, b), c, 0, 0, 0); }
DI unsigned pk(float a, float b) { f32x2 p = {a, b}; return __builtin_bit_cast(unsigned, __builtin_convertvector(p, bf16x2)); }
DI float blo(unsigned u) { return __builtin_bit_cast(float, u << 16); }
DI float bhi(unsigned u) { return __builtin_bit_cast(float, u & 0xFFFF0000u); }
DI float dot2(unsigned a, unsigned b, float c) { return __builtin_amdgcn_fdot2_f32_bf16(__builtin_bit_cast(bf16x2, a), __builtin_bit_cast(bf16x2, b), c, false); }
DI float dsum(v4u a, float c) { return dot2(a.w, ONES, dot2(a.z, ONES, dot2(a.y, ONES, dot2(a.x, ONES, c)))); }
DI float dsq(v4u a, float c) { return dot2(a.w, a.w, dot2(a.z, a.z, dot2(a.y, a.y, dot2(a.x, a.x, c)))); }
DI float fb(unsigned u) { return __builtin_bit_cast(float, u); }
DI unsigned ub(float f) { return __builtin_bit_cast(unsigned, f); }
DI float half_add(float v) { auto r = __builtin_amdgcn_permlane32_swap(ub(v), ub(v), false, false); return fb(r[0]) + fb(r[1]); }
DI float half_max(float v) { auto r = __builtin_amdgcn_permlane32_swap(ub(v), ub(v), false, false); return fmaxf(fb(r[0]), fb(r[1])); }
DI float half_min(float v) { auto r = __builtin_amdgcn_permlane32_swap(ub(v), ub(v), false, false); return fminf(fb(r[0]), fb(r[1])); }
DI f32x16 zero16() { f32x16 z; _Pragma("unroll") for (int i = 0; i < 16; i++) z[i] = 0.f; return z; }
DI v4u ld_sc1(rsrc_t rs, int voff, int soff = 0) { return __builtin_amdgcn_raw_buffer_load_b128(rs, voff, soff, 16); }
DI void st_sc1(v4u v, rsrc_t rs, int voff, int soff = 0) { __builtin_amdgcn_raw_buffer_store_b128(v, rs, voff + soff, 0, 16); }
#define GN "s_nop 0"
#define GP
template <int OFF> DI void glds(rsrc_t rs, int voff, int soff, unsigned lds) {
    asm volatile("s_add_u32 m0, %0, %4\n\t" GN "\n\tbuffer_load_dwordx4 %1, %2, %3 offen lds" ::"s"(lds), "v"(voff), "s"(rs), "s"(soff), "i"(OFF) : "scc", "memory");
}
template <int OFF> DI void glds_sc1(rsrc_t rs, int voff, int soff, unsigned lds) {
    asm volatile("s_add_u32 m0, %0, %4\n\t" GN "\n\tbuffer_load_dwordx4 %1, %2, %3 offen sc1 lds" ::"s"(lds), "v"(voff), "s"(rs), "s"(soff), "i"(OFF) : "scc", "memory");
}
#define PAGE() asm volatile("s_branch 1f\n\t.p2align 12\n1:")
#define SPIN_(x) asm volatile("" : "+s"(x))
#define SPINR(x) do { x = __builtin_amdgcn_readfirstlane(x); asm volatile("" : "+s"(x)); } while (0)
template <int I> struct IC { static constexpr int v = I; };
template <int A, int B, class F> DI void sfor(F f) { if constexpr (A < B) { f(IC<A>{}); sfor<A + 1, B>(f); } }
#define VMCNT(n) asm volatile("s_waitcnt vmcnt(" #n ")" ::: "memory")
DI v4u lds_r128(LAS char* L, int off) { return *(LAS v4u*)(L + off); }
DI void lds_w128(LAS char* L, int off, v4u v) { *(LAS v4u*)(L + off) = v; }
DI void lds_w4f(LAS char* L, int off, const f32x16& a, int g) { *(LAS f32x4*)(L + off) = f32x4{a[4 * g], a[4 * g + 1], a[4 * g + 2], a[4 * g + 3]}; }
DI v4u tr2(LAS char* L, int off0, int off1) {
    s16x4 a = __builtin_amdgcn_ds_read_tr16_b64_v4i16((LAS s16x4*)(L + off0));
    s16x4 b = __builtin_amdgcn_ds_read_tr16_b64_v4i16((LAS s16x4*)(L + off1));
    v2u x = __builtin_bit_cast(v2u, a), y = __builtin_bit_cast(v2u, b);
    return v4u{x.x, x.y, y.x, y.y};
}
DI unsigned umax3(unsigned a, unsigned b, unsigned c) { return max(max(a, b), c); }
DI bool bad1(const v4u& a) { return __builtin_amdgcn_ballot_w64(max(umax3(a.x, a.y, a.z), a.w) == SENT) != 0ull; }
DI bool any_nan(float v) { return __builtin_amdgcn_ballot_w64(v != v) != 0ull; }
template <int N, class Vo, class So> DI void issue(v4u (&A)[N], rsrc_t rs, Vo vo, So so) {
#pragma unroll
    for (int i = 0; i < N; i++) A[i] = ld_sc1(rs, vo(i), so(i));
    SB();
}
template <int I0, int I1, int N> DI bool any_sent_r(const v4u (&A)[N]) {
    unsigned m = 0;
#pragma unroll
    for (int i = I0; i < I1; i++) { m = umax3(m, A[i].x, A[i].y); m = umax3(m, A[i].z, A[i].w); }
    return __builtin_amdgcn_ballot_w64(m == SENT) != 0ull;
}
template <int N> DI bool any_sent(const v4u (&A)[N]) { return any_sent_r<0, N>(A); }
template <int I0, int I1, int N, class Vo, class So> DI void refetch_r(v4u (&A)[N], rsrc_t rs, Vo vo, So so) {
    unsigned sp = 0;
    do {
#pragma unroll
        for (int i = I0; i < I1; i++) {
            const unsigned m = max(umax3(A[i].x, A[i].y, A[i].z), A[i].w);
            asm volatile("v_cmp_eq_u32 vcc, -1, %1\n\ts_nop 4\n\ts_cbranch_vccz 1f\n\tbuffer_load_dwordx4 %0, %2, %3, %4 offen sc1\n1:"
                         : "+v"(A[i]) : "v"(m), "v"(vo(i)), "s"(rs), "s"(so(i)) : "vcc", "memory");
        }
        VMCNT(0);
#pragma unroll
        for (int i = I0; i < I1; i++) PIN(A[i]);
    } while (any_sent_r<I0, I1>(A) && ++sp < SPIN_CAP);
}
template <int N, class Vo, class So> DI void refetch(v4u (&A)[N], rsrc_t rs, Vo vo, So so) { refetch_r<0, N>(A, rs, vo, so); }
DI v4u corr_a(float c) { unsigned h = pk(c, c); float lo = c - blo(h); return v4u{(h & 0xFFFFu) | (pk(lo, lo) << 16), h & 0xFFFFu, 0u, 0u}; }
DI v4u corr_b(float m) { unsigned h = pk(-m, -m); float lo = -m - blo(h); return v4u{h, pk(lo, lo) & 0xFFFFu, 0u, 0u}; }
constexpr float GK1 = -2.f * 0.7978845608028654f * LOG2E, GK2 = GK1 * 0.044715f;
DI float gelu(float x) {
    float e = __builtin_amdgcn_exp2f(x * (x * x * GK2 + GK1));
    return x * __builtin_amdgcn_rcpf(1.f + e);
}
DI float gelu_s(float a, float c1, float c2, float ir) {
    float e = __builtin_amdgcn_exp2f(a * __builtin_fmaf(a * a, c2, c1));
    float y = a * __builtin_amdgcn_rcpf(__builtin_fmaf(e, ir, ir));
    PIN(y);
    return y;
}
struct GeluPipe {
    float t[2], c1, c2, ir;
    v4u u;
    template <int N> DI void op(const f32x16& a) {
        constexpr int p = N / 15, k = N % 15, j = k & 1, st = k >> 1;
        const float x = a[2 * p + j];
        if (k == 14) { u[p & 3] = pk(t[0], t[1]); return; }
        if (st == 0) t[j] = x * x;
        if (st == 1) t[j] = __builtin_fmaf(t[j], c2, c1);
        if (st == 2) t[j] = x * t[j];
        if (st == 3) t[j] = __builtin_amdgcn_exp2f(t[j]);
        if (st == 4) t[j] = __builtin_fmaf(t[j], ir, ir);
        if (st == 5) t[j] = __builtin_amdgcn_rcpf(t[j]);
        if (st == 6) { t[j] = x * t[j]; if (j == 0) PIN(t[j]); }
    }
};
#define NPRE 120
constexpr int gelu_at(int i) {
    int n = NPRE, c = 0;
    while (n < 120 && c < i * 20) { const int st = (n % 15) >> 1; c += (st == 3 || st == 5) ? 8 : 4; n++; }
    return n;
}
DI void add4(f32x4& a, const f32x4& b) {
    float x0 = a[0] + b[0], x1 = a[1] + b[1], x2 = a[2] + b[2], x3 = a[3] + b[3];
    asm volatile("" : "+v"(x0), "+v"(x1), "+v"(x2), "+v"(x3));
    a = f32x4{x0, x1, x2, x3};
}
DI f32x4 gelu4(f32x4 a, float c1, float c2, float ir) {
    const f32x4 y = a * __builtin_elementwise_fma(a * a, f32x4{c2, c2, c2, c2}, f32x4{c1, c1, c1, c1});
    f32x4 e = {__builtin_amdgcn_exp2f(y[0]), __builtin_amdgcn_exp2f(y[1]), __builtin_amdgcn_exp2f(y[2]), __builtin_amdgcn_exp2f(y[3])};
    e = __builtin_elementwise_fma(e, f32x4{ir, ir, ir, ir}, f32x4{ir, ir, ir, ir});
    return a * f32x4{__builtin_amdgcn_rcpf(e[0]), __builtin_amdgcn_rcpf(e[1]), __builtin_amdgcn_rcpf(e[2]), __builtin_amdgcn_rcpf(e[3])};
}
DI float max3f(float a, float b, float c) { return __builtin_elementwise_maximum(__builtin_elementwise_maximum(a, b), c); }
DI float min3f(float a, float b, float c) { return __builtin_elementwise_minimum(__builtin_elementwise_minimum(a, b), c); }
DI void softmax2(f32x16 p, float mul, bool hi, unsigned (&o)[16]) {
#pragma unroll
    for (int i = 12; i < 16; i++) p[i] = hi ? p[0] : p[i];
    float mx = max3f(p[12], p[13], p[14]), mn = min3f(p[12], p[13], p[14]);
#pragma unroll
    for (int i = 0; i < 12; i += 2) { mx = max3f(mx, p[i], p[i + 1]); mn = min3f(mn, p[i], p[i + 1]); }
    mx = max3f(mx, p[15], p[15]); mn = min3f(mn, p[15], p[15]);
    float bx, bn;
    {
        auto a = __builtin_amdgcn_permlane32_swap(ub(mx), ub(mx), false, false);
        auto c = __builtin_amdgcn_permlane32_swap(ub(mn), ub(mn), false, false);
        bx = -max3f(fb(a[0]), fb(a[1]), fb(a[1])) * mul; bn = min3f(fb(c[0]), fb(c[1]), fb(c[1])) * mul;
    }
    f32x2 e[8], f[8];
#pragma unroll
    for (int i = 0; i < 8; i++) {
        const f32x2 q = {p[2 * i], p[2 * i + 1]};
        const f32x2 y = __builtin_elementwise_fma(q, f32x2{mul, mul}, f32x2{bx, bx}), z = __builtin_elementwise_fma(q, f32x2{-mul, -mul}, f32x2{bn, bn});
        e[i] = f32x2{__builtin_amdgcn_exp2f(y[0]), __builtin_amdgcn_exp2f(y[1])};
        f[i] = f32x2{__builtin_amdgcn_exp2f(z[0]), __builtin_amdgcn_exp2f(z[1])};
    }
    const float keep = hi ? 0.f : 1.f;
    f32x2 se = __builtin_elementwise_fma(e[6] + e[7], f32x2{keep, keep}, (e[0] + e[1]) + (e[2] + e[3]) + (e[4] + e[5]));
    f32x2 sf = __builtin_elementwise_fma(f[6] + f[7], f32x2{keep, keep}, (f[0] + f[1]) + (f[2] + f[3]) + (f[4] + f[5]));
    const float re = __builtin_amdgcn_rcpf(half_add(se[0] + se[1])), rf = __builtin_amdgcn_rcpf(half_add(sf[0] + sf[1]));
    const float re2 = re * keep, rf2 = rf * keep;
#pragma unroll
    for (int i = 0; i < 8; i++) {
        o[i] = __builtin_bit_cast(unsigned, __builtin_convertvector(e[i] * (i < 6 ? re : re2), bf16x2));
        o[8 + i] = __builtin_bit_cast(unsigned, __builtin_convertvector(f[i] * (i < 6 ? rf : rf2), bf16x2));
    }
}
template <class Side> DI void softmax2s(f32x16 p, float mul, bool hi, unsigned (&o)[16], Side side) {
#pragma unroll
    for (int i = 12; i < 16; i++) p[i] = hi ? p[0] : p[i];
    float mx = max3f(p[12], p[13], p[14]), mn = min3f(p[12], p[13], p[14]);
#pragma unroll
    for (int i = 0; i < 12; i += 2) { mx = max3f(mx, p[i], p[i + 1]); mn = min3f(mn, p[i], p[i + 1]); }
    mx = max3f(mx, p[15], p[15]); mn = min3f(mn, p[15], p[15]);
    float bx, bn;
    {
        auto a = __builtin_amdgcn_permlane32_swap(ub(mx), ub(mx), false, false);
        auto c = __builtin_amdgcn_permlane32_swap(ub(mn), ub(mn), false, false);
        bx = -max3f(fb(a[0]), fb(a[1]), fb(a[1])) * mul; PIN(bx); bn = min3f(fb(c[0]), fb(c[1]), fb(c[1])) * mul; PIN(bn);
    }
    float e[16], f[16];
#pragma unroll
    for (int i = 0; i < 16; i++) {
        e[i] = __builtin_amdgcn_exp2f(__builtin_fmaf(p[i], mul, bx));
        f[i] = __builtin_amdgcn_exp2f(__builtin_fmaf(p[i], -mul, bn));
    }
    const float keep = hi ? 0.f : 1.f;
    float se = e[12], sf = f[12];
#pragma unroll
    for (int i = 13; i < 16; i++) { se += e[i]; sf += f[i]; asm volatile("" : "+v"(se), "+v"(sf)); }
    se = __builtin_fmaf(se, keep, e[0]); sf = __builtin_fmaf(sf, keep, f[0]);
#pragma unroll
    for (int i = 1; i < 12; i++) { asm volatile("" : "+v"(se), "+v"(sf)); se += e[i]; sf += f[i]; }
    PIN(se); PIN(sf);
    side();
    float re = __builtin_amdgcn_rcpf(half_add(se)), rf = __builtin_amdgcn_rcpf(half_add(sf));
    float re2 = re * keep; PIN(re2); float rf2 = rf * keep; PIN(rf2);
#pragma unroll
    for (int i = 0; i < 16; i++) { e[i] *= i < 12 ? re : re2; f[i] *= i < 12 ? rf : rf2; }
#pragma unroll
    for (int i = 0; i < 16; i += 4) asm volatile("" : "+v"(e[i]), "+v"(e[i + 1]), "+v"(e[i + 2]), "+v"(e[i + 3]), "+v"(f[i]), "+v"(f[i + 1]), "+v"(f[i + 2]), "+v"(f[i + 3]));
#pragma unroll
    for (int i = 0; i < 8; i++) { o[i] = pk(e[2 * i], e[2 * i + 1]); o[8 + i] = pk(f[2 * i], f[2 * i + 1]); }
}
DI void pack_feat(const f32x16& a, bool hi, v4u& f0, v4u& f1) {
    f0 = v4u{pk(a[0], a[1]), pk(a[2], a[3]), pk(a[4], a[5]), pk(a[6], a[7])};
    f1 = v4u{pk(a[8], a[9]), pk(a[10], a[11]), hi ? 0u : pk(a[12], a[13]), hi ? 0u : pk(a[14], a[15])};
}

#define STAMP(i)
#define COUNT(i)

DI v4u wfrag(LAS char* L, int wb0, int wb1, int i) { const int a = ((i & 1) ? wb1 : wb0) + (i >> 1) * 2048; return tr2(L, a, a + 512); }
template <int PD> DI void small_pre(LAS char* L, v4u (&wf)[PD], int wb0, int wb1) {
#pragma unroll
    for (int i = 0; i < PD; i++) wf[i] = wfrag(L, wb0, wb1, i);
    SB();
}
template <int PD, class Side> DI void small_mma(LAS char* L, v4u (&A)[7], v4u (&wf)[PD], int wb0, int wb1, f32x16& a0, f32x16& a1, Side side) {
    a0 = zero16(); a1 = zero16();
#pragma unroll
    for (int i = 0; i < 14; i++) {
        if (i & 1) a1 = mfma32(wf[i % PD], A[i >> 1], a1); else a0 = mfma32(wf[i % PD], A[i >> 1], a0);
        if (i + PD < 14) wf[i % PD] = wfrag(L, wb0, wb1, i + PD);
        side(i);
        SB();
    }
}
template <bool ADD, class Side, class Side2> DI void small_comb(LAS char* L, const f32x16& a0, const f32x16& a1, int w, int l16, f32x4& e, f32x4& od, Side side, Side2 side2) {
#pragma unroll
    for (int g = 1; g < 4; g++) {
        const int o = L_COMB + ((w ^ g) * 3 + (g - 1)) * 2048 + l16;
        lds_w4f(L, o, g < 2 ? a0 : a1, (g & 1) * 2);
        lds_w4f(L, o + 1024, g < 2 ? a0 : a1, (g & 1) * 2 + 1);
    }
    if (ADD) { e += f32x4{a0[0], a0[1], a0[2], a0[3]}; od += f32x4{a0[4], a0[5], a0[6], a0[7]}; }
    else { e = f32x4{a0[0], a0[1], a0[2], a0[3]}; od = f32x4{a0[4], a0[5], a0[6], a0[7]}; }
    int cr = L_COMB + w * 6144 + l16;
    PIN(e); PIN(od); PIN(cr);
    side();
    __syncthreads();
    f32x4 t[6];
#pragma unroll
    for (int g = 0; g < 6; g++) t[g] = *(LAS f32x4*)(L + cr + g * 1024);
    side2();
    SB();
    e = (e + t[0]) + (t[2] + t[4]);
    od = (od + t[1]) + (t[3] + t[5]);
    PIN(e); PIN(od);
}
DI void unpack8n(const v4u& v, f32x4& e, f32x4& od) {
    e = f32x4{blo(v.x), bhi(v.x), blo(v.y), bhi(v.y)};
    od = f32x4{blo(v.z), bhi(v.z), blo(v.w), bhi(v.w)};
}
DI v4u pack8n(const f32x4& e, const f32x4& od) { return v4u{pk(e[0], e[1]), pk(e[2], e[3]), pk(od[0], od[1]), pk(od[2], od[3])}; }
DI void unpack8(const v4u& v, f32x4& e, f32x4& od) {
    e = f32x4{blo(v.x), blo(v.y), blo(v.z), blo(v.w)};
    od = f32x4{bhi(v.x), bhi(v.y), bhi(v.z), bhi(v.w)};
}
DI float row_part(const f32x4& e, const f32x4& od) {
    const f32x4 a = e + od, q = __builtin_elementwise_fma(e, e, od * od);
    const f32x2 a2 = f32x2{a[0], a[1]} + f32x2{a[2], a[3]}, q2 = f32x2{q[0], q[1]} + f32x2{q[2], q[3]};
    auto r = __builtin_amdgcn_permlane32_swap(ub(a2[0] + a2[1]), ub(q2[0] + q2[1]), false, false);
    return fb(r[0]) + fb(r[1]);
}
DI v4u pack8(f32x4 e, f32x4 od) { PIN(e); PIN(od); return v4u{pk(e[0], od[0]), pk(e[1], od[1]), pk(e[2], od[2]), pk(e[3], od[3])}; }

typedef unsigned long long u64x8 __attribute__((ext_vector_type(8)));
typedef unsigned long long u64x2 __attribute__((ext_vector_type(2)));
__global__ void __launch_bounds__(512, 2) __attribute__((aligned(4096))) fused(const u16* x_, const u16* wqkv_, const u16* wphi_, const u16* wo_, const u16* w1_, const u16* w2_, const u16* w3_, const u16* w4_,
                                                u16* out_, unsigned char* ws_) {
    __shared__ __attribute__((aligned(16))) char smem[163840];
    LAS char* L = (LAS char*)smem;
    const unsigned lb = (unsigned)(size_t)L;
    STAMP(0);
    u64x8 ka; u64x2 kb;
    asm volatile("s_load_dwordx16 %0, %2, 0x0\n\ts_load_dwordx4 %1, %2, 0x40" : "=&s"(ka), "=&s"(kb) : "s"(__builtin_amdgcn_kernarg_segment_ptr()));
    const int tid = threadIdx.x, lane = tid & 63;
    int w = __builtin_amdgcn_readfirstlane(tid >> 6);
    asm volatile("s_nop 4" : "+s"(w));
    int b = blockIdx.x;
    unsigned long long pc; unsigned len;
    {
        unsigned k0;
        asm volatile("s_getpc_b64 %0\n.Lhk_pc:\n\ts_mov_b32 %1, .Lhk_end-.Lhk_pc\n\ts_mov_b32 %2, .Lhk_k-.Lhk_pc-512" : "=s"(pc), "=s"(len), "=s"(k0));
        const int o1 = (b >> 3) * 3072 + w * 1024, o2 = ((b >> 3) & 3) * 1024 + k0;
        int o = w < 3 ? o1 : o2;
        asm volatile("" : "+s"(o));
        if (w < 4) glds<L_JUNK>(__builtin_amdgcn_make_buffer_rsrc((void*)pc, 0, len, 0x00020000), lane * 16 + o, 0, lb);
    }
    SB();
    const int l16 = lane * 16;
    const int prt = w & 3;
    volatile LAS unsigned* SY = (volatile LAS unsigned*)(L + L_SY);
    const int head = (b & 1) * 8 + ((b >> 3) & 7);
    int cls = ((b >> 3) & 7) + 2 * (b >> 6); cls = cls >= 7 ? cls - 7 : cls;
    auto chunk = [&](int c) {
        const int base = c / NCLS * NCLS, n = base + NCLS <= 7 ? NCLS : 7 - base, j = c - base + cls;
        return n == 1 ? base : base + (j >= n ? j - n : j);
    };
    int vx0 = (((b & 6) * 2 + (b >> 6)) * 128 + prt * 32 + (lane >> 3)) * 896 + (((lane & 7) ^ (lane >> 4)) << 4), vx1 = (vx0 ^ 64) + 8 * 896;
    int vx2 = vx0 + 16 * 896, vx3 = vx1 + 16 * 896;
    int vq = (prt * 16 + (lane >> 2)) * 2688 + 56 * head - 8 * (head & 1) + (lane & 3) * 16;
    unsigned lx = lb + prt * 4096, lq = lb + prt * 1024;
    int c0x = cls * 128, c0w = cls * 64 * 2688 + 896;
    asm volatile("" : "+v"(vx0), "+v"(vx1), "+v"(vx2), "+v"(vx3), "+v"(vq));
    asm volatile("s_waitcnt lgkmcnt(0)" : "+s"(ka), "+s"(kb), "+s"(lx), "+s"(lq), "+s"(c0x), "+s"(c0w));
    const u16 *x = (const u16*)ka[0], *wqkv = (const u16*)ka[1], *wphi = (const u16*)ka[2], *wo = (const u16*)ka[3], *w1 = (const u16*)ka[4], *w2 = (const u16*)ka[5],
              *w3 = (const u16*)ka[6], *w4 = (const u16*)ka[7];
    u16* out = (u16*)kb[0];
    unsigned char* ws = (unsigned char*)kb[1];
    const rsrc_t rx = __builtin_amdgcn_make_buffer_rsrc((void*)x, 0, S * D * 2, 0x00020000);
    const rsrc_t rwq = __builtin_amdgcn_make_buffer_rsrc((void*)wqkv, 0, D * 3 * D * 2, 0x00020000);
    const rsrc_t rws = __builtin_amdgcn_make_buffer_rsrc(ws, 0, WS_END, 0x00020000);
    auto ldq = [&](auto N) {
        constexpr int n = decltype(N)::v;
        glds<(n < 7 ? L_WK : n < 14 ? L_WV : L_WQ) + (n % 7) * 4096>(rwq, vq, chunk(n % 7) * 64 * 2688 + (n < 7 ? 896 : n < 14 ? 1792 : 0), lq);
    };
    auto ldxa = [&](auto C) { constexpr int c = decltype(C)::v; if constexpr (c < 6) ldq(IC<c + 1>{}); glds<c * 16384>(rx, vx0, chunk(c) * 128, lx); };
    auto ldxb = [&](auto C) {
        constexpr int o = decltype(C)::v * 16384;
        const int cx = chunk(decltype(C)::v) * 128;
        glds<o + 1024>(rx, vx1, cx, lx); glds<o + 2048>(rx, vx2, cx, lx); glds<o + 3072>(rx, vx3, cx, lx);
    };
    if (w >= 4) { glds<L_WK>(rwq, vq, c0w, lq); glds<0>(rx, vx0, c0x, lx); glds<1024>(rx, vx1, c0x, lx); glds<2048>(rx, vx2, c0x, lx); glds<3072>(rx, vx3, c0x, lx); STAMP(63); }
    SB();
    asm volatile("" : "+s"(w));
    if (w == 3) {
        const rsrc_t rph = __builtin_amdgcn_make_buffer_rsrc((void*)(wphi + head * 784), 0, 1568, 0x00020000);
        glds<L_WPHI>(rph, l16, 0, lb); glds<L_WPHI + 1024>(rph, l16 + 1024, 0, lb);
        if (lane < 8) SY[lane] = 0u;
    }
    asm volatile("" : "+s"(b));
    SB();
    const int xcd = b & 7, ii = b >> 3;
    const int rbi = ii >> 3, rb = (xcd >> 1) * 4 + rbi;
    const int s = lane & 31, h = lane >> 5;
    const bool hi = h != 0;
    const bool sact = ii < 28;
    const int gi = (ii * 37) >> 8, grp = xcd * 4 + gi, mem = ii - gi * 7;
    const int kq = w & 3;
    const int iw = xcd * 4 + mem;
    const v4u sent4 = {SENT, SENT, SENT, SENT};
    const int q4 = (lane & 15) >> 2, p4 = lane & 3, blk = (lane >> 4) & 1, g4 = lane >> 4;
    int own = sact ? ((grp * 2 + (w >> 2)) * 28 + mem * 4 + kq) * 1024 + l16 : OOB;
    int sa = sact ? (lane < 16 ? WS_SS + (((w >> 1) * 64 + grp * 2 + (w & 1)) * 7 + mem) * 256 + l16 : OOB) : (iw * 4 + w < 70 && lane < 8) ? WS_CS + (iw * 4 + w) * 128 + l16 : OOB;
    PIN(own); PIN(sa);
    int ax[4];
#pragma unroll
    for (int i = 0; i < 4; i++) { ax[i] = ((prt * 32 + s) * 128 + ((h ^ ((s >> 1) & 7)) << 4)) ^ (i * 32); PIN(ax[i]); }
    int aq = (8 * h + q4) * 64 + 8 * (head & 1) + blk * 32 + p4 * 8;
    PIN(aq);
    STAMP(21);
    __syncthreads();
    SB();

  auto prog = [&](auto RTC) {
    constexpr int rt = decltype(RTC)::v;
    v2u ev;
    if constexpr (rt == 0) {
        asm volatile(".Lhk_k:\n\t" GP "buffer_load_dwordx2 %0, %1, %2, 0 offen sc1" : "=v"(ev) : "v"(w == 3 ? WS_E : OOB), "s"(rws) : "memory");
        st_sc1(sent4, rws, l16, WS_KVP + (head * 16 + rb) * 4096 + w * 1024);
        st_sc1(sent4, rws, sa);
    }
    SB();
    __builtin_amdgcn_s_barrier();
    STAMP(43);
    SB();
    const int RT = grp * 2 + rt;
    unsigned t0, t1;
    unsigned seen = 0;
    auto snap = [&]() { return *(volatile LAS v4u*)SY; };
    auto take = [&](const v4u& r) { seen = (unsigned)__builtin_amdgcn_readfirstlane(min(min(r.x, r.y), min(r.z, r.w))); };
    auto landed = [&](unsigned v) {
        if (seen < v) {
            unsigned sp = SPIN_CAP << 6;
            do { take(snap()); if (SLP && seen < v) __builtin_amdgcn_s_sleep(1); } while (seen < v && --sp);
        }
        asm volatile("" ::: "memory");
    };
    auto k_done = [&](unsigned got, int word = 5) {
        if (__builtin_expect(__builtin_amdgcn_readfirstlane(got) < 4, 0)) {
            unsigned sp = SPIN_CAP << 6;
            while (__builtin_amdgcn_readfirstlane(SY[word]) < 4 && --sp) {}
        }
        asm volatile("" ::: "memory");
    };
    v4u xq[28], wp0, wp1, cb, f0, f1;
    f32x16 acc = zero16();
    float rstd;
    int ia = L_IMG + (prt * 32 + s) * IMGS + h * 8;
    int a_cs = L_CS + w * 256 + lane * 4, a_cr = L_CS + rt * 1024 + lane * 4;
    int a_fa = L_IMG + (8 * g4 + q4) * IMGS + p4 * 8 + (w >> 1) * 32, a_fv = L_IMG + (8 * g4 + q4) * IMGS + p4 * 8 + 128 + (w & 1) * 32;
    int kvo = WS_KVP + (head * 16 + rb) * 4096 + w * 512 + ((g4 >> 1) * 16 + (lane & 15)) * 16 + (g4 & 1) * 8;
    const int ob = WS_O + ((rb * 4 + prt) * 28 + (head >> 2) * 7) * 1024, hq = head & 3;
    int o0 = ob + hq * 1024 + l16, o1 = hi ? OOB : ob + 4096 + hq * 512 + l16, o2 = hi ? ob + 6144 + (hq >> 1) * 512 + s * 16 + (hq & 1) * 8 : OOB;
    auto flags_ok = [&]() {
        const bool ok = f0.x == t0 && f0.y == t1 && f0.z == t0 && f0.w == t1 && f1.x == t0 && f1.y == t1 && f1.z == t0 && f1.w == t1;
        return __builtin_amdgcn_ballot_w64(!ok) == 0ull;
    };
    auto flags_wait = [&](int base) {
        if (__builtin_expect(!flags_ok(), 0)) {
            unsigned sp = 0;
            do {
                __builtin_amdgcn_s_sleep(16);
                asm volatile("" ::: "memory");
                f0 = ld_sc1(rws, lane * 32, base); f1 = ld_sc1(rws, lane * 32 + 16, base);
                COUNT(39);
            } while (!flags_ok() && ++sp < SPIN_CAP);
        }
    };
    auto touch1 = [&](rsrc_t rw, int v) {
        asm volatile("s_add_u32 m0, %0, %3\n\t" GN "\n\tbuffer_load_dword %1, %2, 0 offen lds" ::"s"(lb), "v"(v), "s"(rw), "i"(L_JUNK) : "scc", "memory");
    };
    auto touch2 = [&](int v) {
        asm volatile("s_add_u32 m0, %0, %3\n\t" GN "\n\tbuffer_load_dword %1, %2, 0 offen sc1 lds" ::"s"(lb), "v"(v), "s"(rws), "i"(L_JUNK) : "scc", "memory");
    };
    auto get_wp = [&]() {
        const int a = L_WPHI + (4 * h + q4) * 56 + blk * 32 + p4 * 8;
        wp0 = tr2(L, a, a + 8 * 56);
        wp1 = tr2(L, a + 16 * 56, a + 24 * 56);
        if (hi) { wp1.z = 0; wp1.w = 0; }
        PIN(wp0); PIN(wp1);
    };
    int r7[7], r5[7];
    int m0 = mem + gi * 2; m0 = m0 >= 7 ? m0 - 7 : m0;
    auto tables = [&]() {
#pragma unroll
        for (int i = 0; i < 7; i++) { r7[i] = (i + m0 >= 7 ? i + m0 - 7 : i + m0) * 1024; SPIN_(r7[i]); }
    };
    if constexpr (rt == 0) {
        const bool strad = NOP ? (ws[WS_DBG - 4096] == 77) : ((0x2A54 >> head) & 1);
        unsigned P[3][8];
        int pvo[3], pso[3];
        const int tau0 = (rb * 10 + __builtin_popcount(~0x2A54u & ((1u << head) - 1u))) * 4 + w;
        bool is4[3];
#pragma unroll
        for (int t = 0; t < 3; t++) {
            const int tau = tau0 + t * 640;
            const int g7 = (tau * 9363) >> 16, i = tau - g7 * 7, g = g7 - G_W4;
            is4[t] = t == 2 || (t == 1 && g7 >= G_W4);
            pvo[t] = is4[t] ? (h * 8 + 256 * i + 64 * ((g >> 2) & 3) + 32 * (g & 1) + ((g >> 1) & 1)) * 896 + s * 4 + (g >> 4) * 128
                            : (h * 8 + 112 * (g7 & 3) + 16 * i) * 3584 + s * 4 + (g7 >> 2) * 128;
            pso[t] = WS_WT + g7 * BLK + i * 1024 + l16;
            PIN(pvo[t]); PIN(pso[t]);
        }
        const bool third = tau0 + 1280 < T_END;
        auto p_load = [&](int t) {
            const rsrc_t rw = __builtin_amdgcn_make_buffer_rsrc((void*)(is4[t] ? w4 : w3), 0, D * F * 2, 0x00020000);
            const int ja = is4[t] ? 1792 : 3584;
#pragma unroll
            for (int j = 0; j < 8; j++)
                asm volatile(GP "buffer_load_dword %0, %1, %2, 0 offen" : "=v"(P[t][j]) : "v"(pvo[t] + (j & 3) * ja + (j >> 2) * 14336), "s"(rw) : "memory");
        };
        auto p_store = [&](const unsigned (&Q)[8], int so) {
            v4u e, o;
            asm volatile("v_perm_b32 %0, %9, %8, %16\n\tv_perm_b32 %1, %11, %10, %16\n\tv_perm_b32 %2, %13, %12, %16\n\tv_perm_b32 %3, %15, %14, %16\n\t"
                         "v_perm_b32 %4, %9, %8, %17\n\tv_perm_b32 %5, %11, %10, %17\n\tv_perm_b32 %6, %13, %12, %17\n\tv_perm_b32 %7, %15, %14, %17\n\ts_nop 1"
                         : "=&v"(e[0]), "=&v"(e[1]), "=&v"(e[2]), "=&v"(e[3]), "=&v"(o[0]), "=&v"(o[1]), "=&v"(o[2]), "=&v"(o[3])
                         : "v"(Q[0]), "v"(Q[1]), "v"(Q[2]), "v"(Q[3]), "v"(Q[4]), "v"(Q[5]), "v"(Q[6]), "v"(Q[7]), "s"(0x05040100u), "s"(0x07060302u) : "memory");
            st_sc1(e, rws, so); st_sc1(o, rws, so + 7168);
        };
        int a_st = L_ST + prt * 512 + lane * 4;
        PIN(a_st); PIN(a_cr); PIN(ia); PIN(a_fa); PIN(a_fv); PIN(kvo); PIN(o0); PIN(o1); PIN(o2);
        tables();
        STAMP(1);
        float sm = 0.f, cp[4];
        v4u xa[2][4];
        auto stats = [&](int c) {
#pragma unroll
            for (int i = 0; i < 4; i++) sm = dsum(xa[c & 1][i], sm);
            PIN(sm);
        };
#pragma unroll
        for (int c = 0; c < 7; c++) {
            if (c == 1 || c == 6) PAGE();
            landed(c + 1);
            if (c == 0) STAMP(2);
            if (c == 1) STAMP(46); if (c == 2) STAMP(47); if (c == 3) STAMP(48); if (c == 4) STAMP(49); if (c == 5) STAMP(50); if (c == 6) STAMP(51);
            v4u wk[4], sn;
#pragma unroll
            for (int i = 0; i < 4; i++) {
                xa[c & 1][i] = lds_r128(L, c * 16384 + ax[i]);
                wk[i] = tr2(L, L_WK + c * 4096 + i * 1024 + aq, L_WK + c * 4096 + i * 1024 + 256 + aq);
                SB();
            }
            if (c == 6) {
#pragma unroll
                for (int i = 0; i < 4; i++) cp[i] = *(LAS float*)(L + a_cr + i * 256);
            }
            if (c > 0) stats(c - 1);
            if (c < 6) sn = snap();
            SB();
            if (c == 6) { stats(6); SB(); }
#pragma unroll
            for (int i = 0; i < 4; i++) acc = mfma32(wk[i], xa[c & 1][i], acc);
            if (c < 6) { SB(); take(sn); }
            if (c == 0) {
                VMCNT(0);
                if (w == 3) {
                    unsigned long long tag;
                    asm volatile("v_lshl_add_u64 %0, %1, 0, 1" : "=v"(tag) : "v"(ev) : "memory");
                    SY[8] = (unsigned)tag; SY[9] = (unsigned)(tag >> 32);
                }
                if (!strad) { p_load(0); p_load(1); if (third) p_load(2); }
            }
            if (c == 1) {
                __builtin_amdgcn_s_barrier();
                asm volatile("" ::: "memory");
                if (w == 0) {
                    t0 = SY[8]; t1 = SY[9];
                    __builtin_amdgcn_raw_buffer_store_b64(v2u{t0, t1}, rws, lane == 0 ? WS_FA + b * 8 : OOB, 0, 16);
                }
            }
            if (c == 3) {
                if (lane == 0) __hip_atomic_fetch_add((LAS unsigned*)(L + L_SY + 24), 1u, __ATOMIC_RELAXED, __HIP_MEMORY_SCOPE_WORKGROUP);
                if (!strad) { VMCNT(0); p_store(P[0], pso[0]); }
            }
            if (!strad) {
                if (c == 4) p_store(P[1], pso[1]);
                if (c == 5 && third) p_store(P[2], pso[2]);
            }
            if (c == 2) get_wp();
            if (c < 6) SB();
        }
        float mean = half_add(sm) * (1.f / D);
        PIN(mean);
        cp[0] += cp[1]; PIN(cp[0]); cp[2] += cp[3]; PIN(cp[2]);
        acc = mfma32(corr_a(cp[0] + cp[2]), corr_b(mean), acc);
        *(LAS float*)(L + a_st) = mean;
        asm volatile("" ::: "memory");
        if (lane == 0) __hip_atomic_fetch_add((LAS unsigned*)(L + L_SY + 20), 1u, __ATOMIC_RELAXED, __HIP_MEMORY_SCOPE_WORKGROUP);
        const v4u sn = snap();
        float sq = *(volatile LAS float*)(L + a_st + 256);
        STAMP(3);
        if (w == 0) { f0 = ld_sc1(rws, lane * 32, WS_FA); f1 = ld_sc1(rws, lane * 32 + 16, WS_FA); }
        st_sc1(sent4, rws, o0); st_sc1(sent4, rws, o1);
        __builtin_amdgcn_raw_buffer_store_b64(v2u{SENT, SENT}, rws, o2, 0, 16);
        v4u k0, k1;
        pack_feat(acc, hi, k0, k1);
        const f32x16 pk_ = mfma32(wp1, k1, mfma32(wp0, k0, zero16()));
        st_sc1(sent4, rws, own, WS_H); st_sc1(sent4, rws, own, WS_H + RTB);
        take(sn);
        if (__builtin_expect(seen < 8, 0)) { landed(8); sq = *(volatile LAS float*)(L + a_st + 256); }
        rstd = __builtin_amdgcn_rsqf(fmaxf(__builtin_fmaf(-mean, mean, sq), 0.f) + EPS);
        unsigned ph[16], kd;
        softmax2s(pk_, rstd * LOG2E, hi, ph, [&]() { kd = SY[5]; });
        k_done(kd);
#pragma unroll
        for (int g = 0; g < 8; g++) *(LAS v2u*)(L + ia + g * 16) = v2u{ph[2 * g], ph[2 * g + 1]};
        VMCNT(0);
        if (w == 0) {
            flags_wait(WS_FA);
            if (b == 0 && lane == 0) __builtin_amdgcn_raw_buffer_store_b64(v2u{t0, t1}, rws, WS_E, 0, 16);
        }
        STAMP(24);
    } else {
        int aqo = aq + prt * 1024, a_st = L_ST + prt * 512 + lane * 4;
        float cs = 0.f, sq = 0.f;
        v4u wc[2];
        sfor<0, 7>([&](auto C) {
            constexpr int c = decltype(C)::v;
            if (c == 0) { PIN(ia); PIN(a_fa); PIN(a_fv); PIN(kvo); PIN(a_cr); PIN(a_cs); PIN(aqo); PIN(a_st); tables(); ldq(IC<1>{}); ldxa(IC<1>{}); VMCNT(3); }
            else if constexpr (c < 4) { ldxa(IC<c + 2>{}); VMCNT(7); }
            else if (c == 4) { ldxa(IC<6>{}); PAGE(); VMCNT(6); }
            else if (c == 5) VMCNT(6);
            else VMCNT(3);
            SY[prt] = c + 1;
            if (c == 0) STAMP(2);
            if (c == 1) STAMP(46); if (c == 2) STAMP(47); if (c == 3) STAMP(48); if (c == 4) STAMP(49); if (c == 5) STAMP(50); if (c == 6) STAMP(51);
#pragma unroll
            for (int i = 0; i < 4; i++) xq[4 * c + i] = lds_r128(L, c * 16384 + ax[i]);
            if (c < 6) wc[0] = tr2(L, L_WK + c * 4096 + aqo, L_WK + c * 4096 + 256 + aqo);
            if (c == 5) wc[1] = tr2(L, L_WK + 6 * 4096 + aqo, L_WK + 6 * 4096 + 256 + aqo);
            SB();
            if (c == 0) { ldxb(IC<1>{}); ldxa(IC<2>{}); }
            if constexpr (c < 5) ldxb(IC<c + 2>{});
            if (c == YV) __builtin_amdgcn_s_barrier();
            if (c == 4) { sfor<7, 9>(ldq); STAMP(22); }
            if (c == 5) { *(LAS float*)(L + a_cs - 1024) = dsum(wc[1], dsum(wc[0], cs)); SB(); ldq(IC<9>{}); }
            if (c == 6) ldq(IC<10>{});
            SB();
#pragma unroll
            for (int i = 0; i < 4; i++) sq = dsq(xq[4 * c + i], sq);
            PIN(sq);
            if (c < 5) { cs = dsum(wc[0], cs); PIN(cs); }
            SB();
        });
        sq = half_add(sq) * (1.f / D);
        *(LAS float*)(L + a_st + 256) = sq;
        asm volatile("" ::: "memory");
        SY[prt] = 8;
        const unsigned k3 = SY[6];
        sfor<11, 14>(ldq);
        STAMP(3);
        VMCNT(3);
        SY[prt] = 9;
        v4u wo_[7];
        if (VPRIO) __builtin_amdgcn_s_setprio(2);
        landed(9);
        k_done(k3, 6);
        STAMP(40);
        {
            v4u wf[4], ca, sn;
            float cp[4], mean;
            unsigned kd;
            auto rdv = [&](int i) { return tr2(L, L_WV + i * 1024 + aq, L_WV + i * 1024 + 256 + aq); };
#pragma unroll
            for (int i = 0; i < 4; i++) wf[i] = rdv(i);
            SB();
#pragma unroll
            for (int i = 0; i < 28; i++) {
                acc = mfma32(wf[i & 3], xq[i], acc);
                if (i == 3) STAMP(30); if (i == 6) STAMP(31); if (i == 9) STAMP(37); if (i == 13) STAMP(38); if (i == 15) STAMP(39); if (i == 18) STAMP(42); if (i == 27) STAMP(44);
                if (i == 1) ldq(IC<14>{});
                if (i == 4) {
                    VMCNT(1);
#pragma unroll
                    for (int c = 0; c < 7; c++) wo_[c] = tr2(L, L_WV + c * 4096 + aqo, L_WV + c * 4096 + 256 + aqo);
                }
                if (i == 7) {
                    cs = 0.f;
#pragma unroll
                    for (int c = 0; c < 7; c++) cs = dsum(wo_[c], cs);
                    *(LAS float*)(L + a_cs) = cs; asm volatile("" ::: "memory"); SY[prt] = 10;
                    ldq(IC<15>{});
                }
                if (i == 10) { sn = snap(); kd = SY[5]; }
                if (i == 12) { take(sn); landed(10); }
                if (i == 14) {
                    k_done(kd);
                    mean = *(LAS float*)(L + a_st);
#pragma unroll
                    for (int r = 0; r < 4; r++) cp[r] = *(LAS float*)(L + a_cr + r * 256);
                }
                if (i + 4 < 28) wf[i & 3] = rdv(i + 4);
                if (i == 11) ldq(IC<16>{});
                if (i == 15) ldq(IC<17>{});
                if (i == 19) ldq(IC<18>{});
                if (i == 23) ldq(IC<19>{});
                if (i == 27) ldq(IC<20>{});
                if (i == 19) { cp[0] += cp[1]; PIN(cp[0]); cp[2] += cp[3]; PIN(cp[2]); }
                if (i == 20) { ca = corr_a(cp[0] + cp[2]); PIN(ca); }
                if (i == 22) { cb = corr_b(mean); PIN(cb); }
                if (i == 23) { rstd = __builtin_amdgcn_rsqf(fmaxf(__builtin_fmaf(-mean, mean, sq), 0.f) + EPS); PIN(rstd); }
                if (i == 25) acc = mfma32(ca, cb, acc);
                SB();
            }
        }
#pragma unroll
        for (int g = 0; g < 4; g++) {
            v2u v = {pk(acc[4 * g] * rstd, acc[4 * g + 1] * rstd), pk(acc[4 * g + 2] * rstd, acc[4 * g + 3] * rstd)};
            if (g == 3 && hi) v = v2u{0x00003F80u, 0u};
            *(LAS v2u*)(L + ia + 128 + g * 16) = v;
        }
        if (VPRIO) __builtin_amdgcn_s_setprio(0);
        VMCNT(3);
        STAMP(24);
    }
    __syncthreads();
    {
        v4u fa[4], fv[4];
#pragma unroll
        for (int ks = 0; ks < 4; ks++) {
            fa[ks] = tr2(L, a_fa + ks * 32 * IMGS, a_fa + ks * 32 * IMGS + 4 * IMGS);
            fv[ks] = tr2(L, a_fv + ks * 32 * IMGS, a_fv + ks * 32 * IMGS + 4 * IMGS);
            SB();
        }
        f32x4 kv = {0.f, 0.f, 0.f, 0.f};
#pragma unroll
        for (int ks = 0; ks < 4; ks++) kv = mfma16(fa[ks], fv[ks], kv);
        __builtin_amdgcn_raw_buffer_store_b64(v2u{pk(kv[0], kv[1]), pk(kv[2], kv[3])}, rws, kvo, 0, 16);
        SB();
    }
    STAMP(5);
    const int vrow = ((lane & 7) ^ (((lane >> 4) & 1) << 2)) * 16;
    const rsrc_t rwo = __builtin_amdgcn_make_buffer_rsrc((void*)wo, 0, D * D * 2, 0x00020000);
    int va = sact ? (112 * kq + ((lane >> 3) & 3) + (lane >> 5) * 8) * 896 + mem * 128 + vrow : OOB, vb = va + (lane >> 5) * 20 * 896;
    auto ldo = [&](auto I) {
        constexpr int i = decltype(I)::v, r0 = i < 4 ? 28 * i : i < 6 ? 56 * i - 208 : 20, hs = i < 4 ? 4 : i < 6 ? 28 : 56;
        glds<rt * 1024>(rwo, i < 6 ? va : vb, (r0 + rt * hs) * 896, lb + L_WS + kq * BLK + (i - m0 < 0 ? i - m0 + 7 : i - m0) * 2048);
    };
    int wb0 = L_WS + kq * BLK + (8 * h + q4) * 128 + ((((q4 >> 1) ^ (kq >> 1)) & 1) << 6) + ((blk ^ (kq & 1)) << 5) + (((p4 >> 1) | ((p4 & 1) << 1)) << 3);
    int av = (RT * 28 + kq * 7) * 1024 + l16;
    if constexpr (rt == 0) {
        if (w == 0) __builtin_amdgcn_raw_buffer_store_b64(v2u{t0, t1}, rws, lane == 0 ? WS_F2 + b * 8 : OOB, 0, 16);
        __builtin_amdgcn_s_sleep(6);
        {
            const int v = sact ? (grp * 64 + w * 16 + (lane >> 3)) * 896 + mem * 128 + (((lane & 7) ^ (lane >> 4)) << 4) : OOB;
            glds<L_XR>(rx, v, 0, lb + w * 2048); glds<L_XR + 1024>(rx, v ^ 64, 8 * 896, lb + w * 2048);
        }
        v4u sel = {0u, 0u, 0u, 0u};
        {
            const int j = (lane & 15) - 8 * (g4 & 1);
            const unsigned one = (j & 1) ? 0x3F800000u : 0x00003F80u;
#pragma unroll
            for (int q = 0; q < 4; q++) sel[q] = (j >> 1) == q ? one : 0u;
        }
        int ko = L_KVF + prt * 1024 + ((g4 & 1) * 32 + (lane & 15)) * 16 + (g4 >> 1) * 8;
        int gv = WS_KVP + head * 65536 + prt * 1024 + h * 4096 + s * 16;
        PIN(ko); PIN(sel); PIN(gv); PIN(va); PIN(vb); PIN(wb0); PIN(av);
        PAGE();
        __builtin_amdgcn_s_sleep(GSLEEP);
        STAMP(6);
        v4u G[16];
        auto gvo = [&](int i) { return gv + (i & 1) * 512; };
        auto gso = [&](int i) { return (((i >> 1) + 2 * rbi) & 7) * 8192; };
        issue<16>(G, rws, gvo, gso);
        f32x4 k0 = {0.f, 0.f, 0.f, 0.f}, k1 = k0;
#pragma unroll
        for (int i = 0; i < 8; i++) { k0 = mfma16(sel, G[2 * i], k0); k1 = mfma16(sel, G[2 * i + 1], k1); }
        STAMP(7);
        if (any_nan(k0[0] + k1[0])) {
            COUNT(33);
            refetch<16>(G, rws, gvo, gso);
            k0 = f32x4{0.f, 0.f, 0.f, 0.f}; k1 = k0;
#pragma unroll
            for (int i = 0; i < 8; i++) { k0 = mfma16(sel, G[2 * i], k0); k1 = mfma16(sel, G[2 * i + 1], k1); }
        }
        *(LAS v2u*)(L + ko) = v2u{pk(k0[0], k0[1]), pk(k0[2], k0[3])};
        *(LAS v2u*)(L + ko + 256) = v2u{pk(k1[0], k1[1]), pk(k1[2], k1[3])};
        STAMP(8);
        __syncthreads();
        if (w == 0) {
            f0 = ld_sc1(rws, lane * 32, WS_F2); f1 = ld_sc1(rws, lane * 32 + 16, WS_F2);
        }
        sfor<0, 5>(ldo);
        if (w >= 2) touch1(__builtin_amdgcn_make_buffer_rsrc((void*)w1, 0, D * D * 2, 0x00020000), lane < 49 ? (ii * 98 + (w - 2) * 49 + lane) * 128 : OOB);
    } else {
        PAGE();
        STAMP(41);
        {
            v4u wf[4], ca, sn, wc[7];
            float cp[4];
            const int aqo = L_WQ + aq + prt * 1024;
            auto rdq = [&](int i) { return tr2(L, L_WQ + i * 1024 + aq, L_WQ + i * 1024 + 256 + aq); };
#pragma unroll
            for (int i = 0; i < 4; i++) wf[i] = rdq(i);
            SB();
            acc = zero16();
#pragma unroll
            for (int i = 0; i < 28; i++) {
                acc = mfma32(wf[i & 3], xq[i], acc);
                if (i == 0) STAMP(23); if (i == 3) STAMP(10); if (i == 6) STAMP(45); if (i == 9) STAMP(4); if (i == 14) STAMP(60); if (i == 18) STAMP(61);
                if (i == 4) {
                    VMCNT(1);
#pragma unroll
                    for (int c = 0; c < 7; c++) wc[c] = tr2(L, c * 4096 + aqo, c * 4096 + 256 + aqo);
                }
                if (i == 7) {
                    float cs = 0.f;
#pragma unroll
                    for (int c = 0; c < 7; c++) cs = dsum(wc[c], cs);
                    *(LAS float*)(L + a_cs - 1024) = cs; asm volatile("" ::: "memory"); SY[prt] = 11;
                }
                if (i == 16) get_wp();
                if (i == 10) sn = snap();
                if (i == 12) {
                    take(sn); landed(11);
#pragma unroll
                    for (int r = 0; r < 4; r++) cp[r] = *(LAS float*)(L + a_cr - 1024 + r * 256);
                }
                if (i + 4 < 28) wf[i & 3] = rdq(i + 4);
                if (i == 22) { cp[0] += cp[1]; PIN(cp[0]); cp[2] += cp[3]; PIN(cp[2]); }
                if (i == 23) { ca = corr_a(cp[0] + cp[2]); PIN(ca); }
                if (i == 25) acc = mfma32(ca, cb, acc);
                SB();
            }
        }
        STAMP(6);
        if (w >= 6) touch1(rwo, lane < 49 ? (ii * 98 + (w - 6) * 49 + lane) * 128 : OOB);
        v4u q0, q1;
        pack_feat(acc, hi, q0, q1);
        unsigned ph[16];
        softmax2(mfma32(wp1, q1, mfma32(wp0, q0, zero16())), rstd * LOG2E, hi, ph);
#pragma unroll
        for (int i = 0; i < 16; i++) PIN(ph[i]);
        PIN(o0); PIN(o1); PIN(o2); PIN(va); PIN(vb); PIN(wb0); PIN(av);
        STAMP(8);
        __syncthreads();
        v4u fa[4];
#pragma unroll
        for (int f = 0; f < 4; f++) fa[f] = lds_r128(L, L_KVF + f * 1024 + l16);
        f32x16 o = zero16();
#pragma unroll
        for (int f = 0; f < 4; f++) o = mfma32(fa[f], v4u{ph[4 * f], ph[4 * f + 1], ph[4 * f + 2], ph[4 * f + 3]}, o);
        auto r = __builtin_amdgcn_permlane32_swap(ub(o[12]), ub(o[12]), false, false);
        const float rd = __builtin_amdgcn_rcpf(fb(r[1]) + 1e-6f);
        st_sc1(v4u{pk(o[0] * rd, o[1] * rd), pk(o[2] * rd, o[3] * rd), pk(o[4] * rd, o[5] * rd), pk(o[6] * rd, o[7] * rd)}, rws, o0);
        const v4u u = {pk(o[8] * rd, o[9] * rd), pk(o[10] * rd, o[11] * rd), pk(o[12] * rd, o[13] * rd), pk(o[14] * rd, o[15] * rd)};
        st_sc1(u, rws, o1);
        __builtin_amdgcn_raw_buffer_store_b64(v2u{u.x, u.y}, rws, o2, 0, 16);
        SB();
    }
    STAMP(9);
    const unsigned lw7 = lb + L_WS + (w >> 1) * BLK + (w & 1) * 1024;
    int vsw = (112 * (w >> 1) + 8 * (w & 1) + (lane >> 3)) * 896 + vrow + (sact ? mem : iw) * 128;
    const rsrc_t rw1 = __builtin_amdgcn_make_buffer_rsrc((void*)w1, 0, D * D * 2, 0x00020000);
    const rsrc_t rw2 = __builtin_amdgcn_make_buffer_rsrc((void*)w2, 0, D * D * 2, 0x00020000);
    auto ldw1 = [&](auto I) { constexpr int i = decltype(I)::v; glds<i * 2048>(rw1, vsw, r7[i] * 14, lw7); };
    auto ldw2 = [&](auto I) { constexpr int i = decltype(I)::v; glds<i * 2048>(rw2, vsw, r7[i] * 14, lw7); };
    v4u W3[28];
    if (__builtin_expect(!sact, 0)) {
        const unsigned lw1 = lb + w * 1024;
        VMCNT(0);
        __syncthreads();
        if (iw < 7) sfor<0, 7>(ldw1);
        if (iw < 7) {
            VMCNT(0);
            __syncthreads();
            const int a = L_WS + kq * BLK + (8 * h + q4) * 128 + ((((q4 >> 1) ^ rt) & 1) << 6) + blk * 32 + p4 * 8;
            float c = 0.f;
#pragma unroll
            for (int i = 0; i < 7; i++) c = dsum(tr2(L, a + i * 2048, a + i * 2048 + 512), c);
            *(LAS float*)(L + a_cs) = half_add(c);
            __syncthreads();
            if (kq == 0) {
                const float v = (*(LAS float*)(L + a_cr) + *(LAS float*)(L + a_cr + 256)) + (*(LAS float*)(L + a_cr + 512) + *(LAS float*)(L + a_cr + 768));
                __builtin_amdgcn_raw_buffer_store_b32(ub(v), rws, hi ? OOB : WS_CS + iw * 256 + rt * 128 + s * 4, 0, 16);
            }
        }
        auto code = [&]() { if (RECODE) touch1(__builtin_amdgcn_make_buffer_rsrc((void*)pc, 0, len, 0x00020000), lane < 24 ? ((mem * 8 + w) * 24 + lane) * 128 : OOB); };
        code();
#pragma unroll
        for (int i = 0; i < 7; i++) st_sc1(sent4, rws, l16, WS_T + iw * 2 * RTB + w * 7168 + i * 1024);
        VMCNT(0);
        __syncthreads();
        if (rt == 0 && w == 0) __builtin_amdgcn_raw_buffer_store_b64(v2u{t0, t1}, rws, lane == 0 ? WS_F3 + 8192 + iw * 128 : OOB, 0, 16);
        STAMP(52);
#pragma unroll
        for (int i = 0; i < 7; i++) st_sc1(sent4, rws, l16, WS_X1 + iw * 2 * RTB + w * 7168 + i * 1024);
        VMCNT(0);
        if (rt == 0 && w == 0) flags_wait(WS_F2);
        __syncthreads();
        if (rt == 0 && w == 0) __builtin_amdgcn_raw_buffer_store_b64(v2u{t0, t1}, rws, lane == 0 ? WS_F3 + iw * 256 : OOB, 0, 16);
        STAMP(53);
        code();
        const int ct = w * 32 + 31 - iw;
        if (rt == 0 && ct < 56) {
            const int vo = WS_WT + (G_W3 + (ct >> 1) * 4) * BLK + (ct & 1) * 7168 + l16;
            issue<28>(W3, rws, [&](int i) { return vo; }, [&](int i) { return (i / 7) * BLK + (i % 7) * 1024; });
            float c = 0.f;
#pragma unroll
            for (int i = 0; i < 28; i++) c = dsum(W3[i], c);
            __builtin_amdgcn_raw_buffer_store_b32(ub(half_add(c)), rws, hi ? OOB : WS_CS + (14 + ct) * 128 + s * 4, 0, 16);
        }
#pragma unroll 4
        for (int i = 0; i < 28; i++) st_sc1(sent4, rws, l16, WS_U + iw * 8 * RTB + w * RTB + i * 1024);
        VMCNT(0);
        __syncthreads();
        if (rt == 0 && w == 0) __builtin_amdgcn_raw_buffer_store_b64(v2u{t0, t1}, rws, lane == 0 ? WS_F3 + iw * 256 + 128 : OOB, 0, 16);
        STAMP(54);
        code();
        VMCNT(0);
        if constexpr (rt == 0) asm volatile(".Lhk_end:");
        return;
    }
    if constexpr (rt == 0) { __builtin_amdgcn_s_barrier(); sfor<5, 7>(ldo); }
    else { __builtin_amdgcn_s_barrier(); sfor<0, 7>(ldo); }
    int v3 = WS_WT + (G_W3 + (mem * 4 + (w >> 1)) * 4) * BLK + (w & 1) * 7168 + l16;
    auto ld3x = [&](int i) { W3[i] = ld_sc1(rws, v3, r7[i % 7] + (i / 7) * BLK); };
    auto ld3 = ld3x;
    auto ld3r = [&](int i0, int i1) { _Pragma("unroll") for (int i = i0; i < i1; i++) ld3(i); };
    int v4 = WS_WT + (G_W4 + mem * 16 + w * 2) * BLK + ((lane ^ (kq << 3)) << 4);
    int uo = WS_U + grp * 8 * RTB + w * BLK + mem * 1024 + l16;
    const int wb1 = wb0 ^ 64;
    const unsigned lw6 = lb + L_COMB + w * 6144, lw5 = lb + L_W4B + w * 5120;
    auto s7 = [&](int i) { return r7[i]; };
    const int sw = L_ST + w * 256 + lane * 4, sv = WS_SS + (RT * 7 + kq) * 256 + lane * 4;
    v4u hres, W4[17];
    f32x16 a0, a1;
    f32x4 e, od;
    auto s4o = [&](int i) { const int k = i >> 1; return (k < 2 ? r5[0] + k * BLK : k < 8 ? r5[k - 1] : r5[k - 7] + BLK) + (i & 1) * 7168; };
    auto ld4l = [&](auto I, auto DV) {
        constexpr int i = decltype(I)::v, dv = decltype(DV)::v;
        glds_sc1<(i >> 1) * 1024 + dv * 4 * ((i & 1) ? 5120 : 6144)>(rws, v4 + dv * 8 * BLK, s4o(i), (i & 1) ? lw5 : lw6);
    };
    auto ld4 = [&](int i) { W4[i - 11] = ld_sc1(rws, v4, s4o(i)); };
    auto stage_in = [&](v4u (&A)[7], int vo, bool first = false) {
        PAGE();
        VMCNT(0);
        if (!rt && first && w == 0) flags_wait(WS_F2);
        __builtin_amdgcn_s_barrier();
        STAMP(55);
        issue<7>(A, rws, [&](int) { return vo; }, s7);
        STAMP(56);
        asm volatile("" ::: "memory");
    };
    v2u f3;
    auto armed_ld = [&](int k) {
        if (!rt) f3 = __builtin_amdgcn_raw_buffer_load_b64(rws, w != 0 ? OOB : k < 2 ? WS_F3 + grp * 256 + k * 128 : WS_F3 + 8192 + min(max(grp - 1 + min(lane, 2), 0), 31) * 128, 0, 16);
    };
    auto armed_wait = [&](int k) {
        if (!rt && w == 0) {
            unsigned sp = 0;
            while (__builtin_amdgcn_ballot_w64(!(f3.x == t0 && f3.y == t1)) != 0ull && ++sp < SPIN_CAP) { asm volatile("" ::: "memory"); COUNT(32); armed_ld(k); }
        }
    };
    auto stage_mma = [&](v4u (&A)[7], v4u (&wf)[4], int vo, int p0) {
        auto fv = [&](int) { return vo; };
        if (any_sent_r<0, CHK>(A)) { COUNT(34); refetch_r<0, CHK>(A, rws, fv, s7); }
        SB();
        STAMP(14);
        small_mma<4>(L, A, wf, wb0, wb1, a0, a1, [&](int i) {
            if (CHK < 7 && i == 2 * CHK - 1 && any_sent_r<CHK, 7>(A)) { COUNT(37); refetch_r<CHK, 7>(A, rws, fv, s7); }
            if (rt && i % 3 == 1 && PFY + i / 3 < 7) ld3(p0 + PFY + i / 3);
        });
    };
    auto sums_out = [&](int buf) {
        *(LAS float*)(L + sw) = row_part(e, od);
        __syncthreads();
    };
    auto sums_out2 = [&](int buf) {
        if (kq == 0) {
            const float v = (*(LAS float*)(L + sw) + *(LAS float*)(L + sw + 256)) + (*(LAS float*)(L + sw + 512) + *(LAS float*)(L + sw + 768));
            __builtin_amdgcn_raw_buffer_store_b32(ub(v), rws, sv + buf * (64 * 7 * 256) + mem * 256, 0, 16);
        }
        SB();
    };
    unsigned z0, z1;
    auto sums_ld = [&](int buf) {
        z0 = __builtin_amdgcn_raw_buffer_load_b32(rws, sv, buf * (64 * 7 * 256), 16);
        z1 = __builtin_amdgcn_raw_buffer_load_b32(rws, kq == 3 ? OOB : sv + 1024, buf * (64 * 7 * 256), 16);
    };
    auto sums_in = [&](int buf) {
        if (__builtin_expect(__builtin_amdgcn_ballot_w64(max(z0, z1) == SENT) != 0ull, 0)) {
            unsigned sp = 0;
            COUNT(38);
            do { asm volatile("" ::: "memory"); sums_ld(buf); } while (__builtin_amdgcn_ballot_w64(max(z0, z1) == SENT) != 0ull && ++sp < SPIN_CAP);
        }
        *(LAS float*)(L + a_cs) = fb(z0) + fb(z1);
    };
    {
        v4u A[7], wf[4];
        const int vo = av + WS_O;
        unpack8n(lds_r128(L, L_XR + (rt * 32 + s) * 128 + (((2 * kq + h) ^ ((s >> 1) & 7)) << 4)), e, od);
        PIN(e); PIN(od); PIN(v3); PIN(v4); PIN(uo); PIN(vsw);
        stage_in(A, vo, true);
        if (rt) ld3r(0, PFY);
        small_pre<4>(L, wf, wb0, wb1);
        STAMP(25);
        stage_mma(A, wf, vo, 0);
        small_comb<true>(L, a0, a1, w, l16, e, od, [&]() {
            if (!rt) { if (w >= 2) touch1(rw2, lane < 56 ? (ii * 112 + (w - 2) * 56 + lane) * 128 : OOB); ld3r(0, 7); }
        }, [&]() { if (EARLYW & 1) sfor<0, 2>(ldw1); });
        hres = pack8n(e, od);
        st_sc1(hres, rws, own, WS_H);
        SB();
    }
    STAMP(11);
    {
        if (EARLYW & 1) sfor<2, 4>(ldw1);
        sums_out(0);
        sums_out2(0);
        if (EARLYW & 1) sfor<4, 7>(ldw1); else sfor<0, 7>(ldw1);
        v4u A[7], wf[4], C[2];
        const int vo = av + WS_H, cv = WS_CS + mem * 256 + kq * 64 + h * 32;
        stage_in(A, vo);
        sums_ld(0);
        armed_ld(2);
        C[0] = ld_sc1(rws, cv); C[1] = ld_sc1(rws, cv + 16);
        if (rt) ld3r(7, 7 + PFY);
        small_pre<4>(L, wf, wb0, wb1);
        STAMP(12);
        stage_mma(A, wf, vo, 7);
        STAMP(26);
        float st[4];
        small_comb<false>(L, a0, a1, w, l16, e, od, [&]() {
            sums_in(0);
            armed_wait(2);
            if (__builtin_expect(any_sent<2>(C), 0)) refetch<2>(C, rws, [&](int i) { return cv + i * 16; }, [&](int) { return 0; });
            if (!rt) ld3r(7, 14);
        }, [&]() {
#pragma unroll
            for (int i = 0; i < 4; i++) st[i] = *(LAS float*)(L + a_cr + i * 256);
            if ((EARLYW & 2) && rt) sfor<0, 3>(ldw2);
        });
        const float sx = ((st[0] + st[1]) + (st[2] + st[3])) * (1.f / D);
        auto sr = __builtin_amdgcn_permlane32_swap(ub(sx), ub(sx), false, false);
        const float m = fb(sr[0]);
        const float var = fmaxf(__builtin_fmaf(-m, m, fb(sr[1])), 0.f);
        const float v2 = __builtin_fmaf(var + EPS, EPS, var);
        const float sc = __builtin_amdgcn_rsqf(v2), c1 = GK1 * sc, c2 = 0.044715f * c1 * sc * sc, ir = v2 * sc;
        e = __builtin_elementwise_fma(__builtin_bit_cast(f32x4, C[0]), f32x4{-m, -m, -m, -m}, e);
        od = __builtin_elementwise_fma(__builtin_bit_cast(f32x4, C[1]), f32x4{-m, -m, -m, -m}, od);
        st_sc1(pack8n(gelu4(e, c1, c2, ir), gelu4(od, c1, c2, ir)), rws, own, WS_T);
        SB();
        STAMP(13);
        if (EARLYW & 2) { if (!rt) sfor<0, 4>(ldw2); __builtin_amdgcn_s_barrier(); if (rt) sfor<3, 7>(ldw2); else sfor<4, 7>(ldw2); } else { __builtin_amdgcn_s_barrier(); sfor<0, 7>(ldw2); }
        STAMP(32);
        const float r1 = __builtin_amdgcn_rsqf(var + EPS);
        unpack8n(hres, e, od);
        e = (e - m) * r1; od = (od - m) * r1;
        PIN(e); PIN(od);
    }
    {
        v4u A[7], wf[4];
        const int g = RT * 32 + s + (kq == 0 ? -1 : kq == 1 ? 1 : kq == 2 ? -2 : 2);
        const int vo = (g >= 0 && g < S) ? WS_T + (g >> 5) * RTB + kq * 7168 + (h * 32 + (g & 31)) * 16 : OOB;
        stage_in(A, vo);
        armed_ld(0);
        if (rt) ld3r(14, 14 + PFY);
        small_pre<4>(L, wf, wb0, wb1);
        STAMP(27);
        stage_mma(A, wf, vo, 14);
        STAMP(33);
        small_comb<true>(L, a0, a1, w, l16, e, od, [&]() {
            STAMP(34);
            armed_wait(0);
            if (!rt) ld3r(14, 21);
            STAMP(35);
        }, [&]() { STAMP(36); });
        const v4u x1 = pack8n(e, od);
        st_sc1(x1, rws, own, WS_X1);
        SB();
        STAMP(15);
        sums_out(1);
        sums_out2(1);
        ld3r(21, 28);
        STAMP(62);
#pragma unroll
        for (int i = 0; i < 7; i++) { r5[i] = (i + mem >= 7 ? i + mem - 7 : i + mem) * 1024; SPIN_(r5[i]); }
        f32x4 he, ho;
        unpack8(x1, e, od); unpack8(hres, he, ho);
        e += he; od += ho;
        PIN(e); PIN(od);
    }
    v4u A[14], B[14];
    const int ub0 = uo - mem * 1024;
    auto us = [&](int k) { return k < 2 ? r5[0] + k * 7168 : k < 8 ? r5[k - 1] : r5[k - 7] + 7168; };
    GeluPipe g0, g1;
    f32x16 a2;
    {
        v4u X[7];
        const int vo = av + WS_X1, cv = hi ? OOB : WS_CS + (14 + mem * 8 + w) * 128 + s * 4;
        unsigned c3 = __builtin_amdgcn_raw_buffer_load_b32(rws, cv, 0, 16);
        int xw = L_WS + w * 7168 + l16, sr = L_CS + lane * 4;
        PIN(xw); PIN(sr);
        __builtin_amdgcn_s_barrier();
        issue<7>(X, rws, [&](int) { return vo; }, s7);
        sums_ld(1);
        armed_ld(1);
        if (rt && XP) sfor<0, XP>([&](auto I) { ld4(11 + decltype(I)::v); });
        SB();
        if (__builtin_expect(__builtin_amdgcn_ballot_w64(c3 == SENT) != 0ull, 0)) {
            unsigned sp = 0;
            do {
                asm volatile("" ::: "memory");
                c3 = __builtin_amdgcn_raw_buffer_load_b32(rws, cv, 0, 16);
            } while (__builtin_amdgcn_ballot_w64(c3 == SENT) != 0ull && ++sp < SPIN_CAP);
        }
        v4u ca = corr_a(fb(c3));
        PIN(ca);
        STAMP(16);
        unsigned mx = 0;
#pragma unroll
        for (int i = 0; i < 7; i++) { lds_w128(L, xw + i * 1024, X[i]); mx = umax3(mx, X[i].x, X[i].y); mx = umax3(mx, X[i].z, X[i].w); SB(); }
        if (__builtin_expect(__builtin_amdgcn_ballot_w64(mx == SENT) != 0ull, 0)) {
            COUNT(35);
            refetch<7>(X, rws, [&](int) { return vo; }, s7);
#pragma unroll
            for (int i = 0; i < 7; i++) lds_w128(L, xw + i * 1024, X[i]);
        }
        sums_in(1);
        armed_wait(1);
        if (!rt) {
#pragma unroll
            for (int i = 11; i < 19; i++) ld4(i);
            PAGE();
        }
        __syncthreads();
        STAMP(28);
        if (rt) {
            sfor<0, 11>([&](auto I) { ld4l(I, IC<0>{}); });
            sfor<0, XP>([&](auto I) { ld4l(I, IC<-1>{}); });
#pragma unroll
            for (int i = 11 + XP; i < 15; i++) ld4(i);
            SB();
            PAGE();
        }
        __builtin_amdgcn_s_setprio(3);
        v4u af[4];
#pragma unroll
        for (int i = 0; i < 4; i++) af[i] = lds_r128(L, L_WS + i * 1024 + l16);
        v4u cbx[2];
        {
            float s0[2], s1[2], st[8];
            a0 = zero16();
#pragma unroll
            for (int i = 0; i < 28; i++) {
                a0 = mfma32(W3[i], af[i & 3], a0);
                if (i < 4) { st[2 * i] = *(LAS float*)(L + sr + (2 * i) * 256); st[2 * i + 1] = *(LAS float*)(L + sr + (2 * i + 1) * 256); }
                af[i & 3] = lds_r128(L, L_WS + (i + 4) * 1024 + l16);
                const int r2 = i >= 16, k = i - 8 * r2;
                GeluPipe& g = r2 ? g1 : g0;
                if (k == 8) { s0[r2] = st[4 * r2] + st[4 * r2 + 1]; PIN(s0[r2]); s0[r2] += st[4 * r2 + 2]; PIN(s0[r2]); s0[r2] = (s0[r2] + st[4 * r2 + 3]) * (1.f / D); PIN(s0[r2]); }
                if (k == 9) { auto q = __builtin_amdgcn_permlane32_swap(ub(s0[r2]), ub(s0[r2]), false, false); s0[r2] = fb(q[0]); s1[r2] = fb(q[1]); PIN(s0[r2]); PIN(s1[r2]); }
                if (k == 10) { s1[r2] = fmaxf(__builtin_fmaf(-s0[r2], s0[r2], s1[r2]), 0.f) + EPS; PIN(s1[r2]); }
                if (k == 11) { cbx[r2] = corr_b(s0[r2]); PIN(cbx[r2]); }
                if (k == 13) { g.c1 = __builtin_amdgcn_rsqf(s1[r2]); PIN(g.c1); g.ir = s1[r2] * g.c1; PIN(g.ir); g.c2 = g.c1 * g.c1; PIN(g.c2); }
                if (k == 14) { g.c1 *= GK1; PIN(g.c1); g.c2 *= 0.044715f; PIN(g.c2); g.c2 *= g.c1; PIN(g.c2); }
                if (i == 15) a0 = mfma32(ca, cbx[0], a0);
                SB();
            }
        }
        __builtin_amdgcn_s_setprio(2);
        STAMP(29);
        auto g0op = [&](auto N) {
            constexpr int n = decltype(N)::v;
            g0.template op<n>(a0);
            if (n % 60 == 59) { st_sc1(g0.u, rws, uo + (n / 60) * 7168); A[n / 60] = g0.u; }
        };
        sfor<0, NPRE>([&](auto N) {
            constexpr int n = decltype(N)::v, k = n / 5;
            if constexpr (n % 5 == 0 && k < (rt ? 7 : 14) && (rt || k >= XP)) {
                SB();
                if constexpr (rt) ld4(15 + k); else if constexpr (k < 11) ld4l(IC<k>{}, IC<0>{}); else ld4(8 + k);
                SB();
            }
            g0op(N);
        });
        SB();
        STAMP(57);
        a2 = mfma32(ca, cbx[1], zero16());
        sfor<0, 28>([&](auto I) {
            constexpr int i = decltype(I)::v;
            a2 = mfma32(W3[i], af[i & 3], a2);
            SB();
            if (i + 4 < 28) af[i & 3] = lds_r128(L, L_WS + (28 + i + 4) * 1024 + l16);
            sfor<gelu_at(i), gelu_at(i + 1)>(g0op);
            if (i >= 4 && i < 16 && !(i & 1)) ld4(22 + (i - 4) / 2);
            if (i >= 28 - 2 * EQ && !(i & 1)) A[2 + (i - 28 + 2 * EQ) / 2] = ld_sc1(rws, ub0, us(2 + (i - 28 + 2 * EQ) / 2));
            SB();
        });
        __builtin_amdgcn_s_setprio(1);
        PIN(a2);
        if (lane == 0) __hip_atomic_fetch_add((LAS unsigned*)(L + L_SY + 16), 1u, __ATOMIC_RELAXED, __HIP_MEMORY_SCOPE_WORKGROUP);
        STAMP(17);
    }
    {
        auto g1op = [&](auto N) {
            constexpr int n = decltype(N)::v;
            g1.template op<n>(a2);
            if (n % 60 == 59) { st_sc1(g1.u, rws, uo + 4 * RTB + (n / 60) * 7168); B[n / 60] = g1.u; }
        };
        sfor<0, 120>([&](auto N) {
            constexpr int n = decltype(N)::v, k = n / ASP;
            if constexpr (n % ASP == 0 && k >= EQ && k < 12) { SB(); A[k + 2] = ld_sc1(rws, ub0, us(k + 2)); SB(); }
            g1op(N);
        });
        SB();
        STAMP(59);
        int p5 = L_W4B + w * 5120 + l16, pw[4], cr = L_COMB + w * 6144 + l16;
#pragma unroll
        for (int g = 1 - rt; g < 4; g++) { pw[g] = ((g ^ kq) * 7 + rt * 4 + g - 1) * 2048 + l16; PIN(pw[g]); }
        PIN(p5); PIN(cr);
        PAGE();
        VMCNT(13);
        auto w4f = [&](int i) { return i >= 11 ? W4[i - 11] : lds_r128(L, ((i & 1) ? p5 : cr) + (i >> 1) * 1024); };
        unsigned cm;
        auto check = [&](auto I, v4u (&U)[14], int r2) {
            constexpr int i = decltype(I)::v, q = i >= 8, k = i - 12 * q;
            if constexpr (k >= 1 && k < 4) {
                constexpr int j = 2 * k + 6 * q;
                cm = umax3(k == 1 ? 0u : cm, U[j].x, U[j].y); cm = umax3(cm, U[j].z, U[j].w); cm = umax3(cm, U[j + 1].x, U[j + 1].y); cm = umax3(cm, U[j + 1].z, U[j + 1].w);
                if (k < 3) PIN(cm);
                if (k == 3 && __builtin_amdgcn_ballot_w64(cm == SENT) != 0ull) { COUNT(36); refetch_r<2 + 6 * q, 8 + 6 * q>(U, rws, [&](int) { return ub0 + r2 * 4 * RTB; }, us); }
            }
        };
        int qa[7], qb[7], xo;
        f32x4 t[14];
        v4u wl[4];
        unsigned xd;
#pragma unroll
        for (int i = 0; i < 4; i++) wl[i] = w4f(i);
        SB();
        a0 = zero16(); a1 = zero16();
        sfor<0, 28>([&](auto I) {
            constexpr int i = decltype(I)::v;
            const v4u wv = i < 11 ? wl[i & 3] : W4[i < 11 ? 0 : i - 11];
            if (i & 1) a1 = mfma32(wv, A[i >> 1], a1); else a0 = mfma32(wv, A[i >> 1], a0);
            SB();
            if (i + 4 < 11) wl[i & 3] = w4f(i + 4);
            check(I, A, 0);
            if (i >= 28 - 2 * EQ && !(i & 1)) B[2 + (i - 28 + 2 * EQ) / 2] = ld_sc1(rws, ub0, us(2 + (i - 28 + 2 * EQ) / 2) + 4 * RTB);
            if (i == 20) xd = SY[4];
            SB();
        });
        STAMP(18);
        __builtin_amdgcn_s_setprio(0);
        if (__builtin_expect(__builtin_amdgcn_readfirstlane(xd) < 8, 0)) {
            unsigned sp = SPIN_CAP << 6;
            while (__builtin_amdgcn_readfirstlane(SY[4]) < 8 && --sp) __builtin_amdgcn_s_sleep(1);
        }
        asm volatile("" ::: "memory");
#pragma unroll
        for (int k = 2; k < 14; k++) {
            if (k >= 2 + EQ) B[k] = ld_sc1(rws, ub0, us(k) + 4 * RTB);
            if (k >= 4 - 2 * rt && k < 10) lds_w4f(L, pw[(k >> 1) - 1] + (k & 1) * 1024, (k & 1) ? a1 : a0, (k >> 1) - 1);
            if (k >= 10) wl[k - 10] = w4f(k - 10);
            if (rt == 0 && k == 10) add4(e, f32x4{a0[0], a0[1], a0[2], a0[3]});
            if (rt == 0 && k == 11) add4(od, f32x4{a1[0], a1[1], a1[2], a1[3]});
            SB();
        }
        if (rt) {
#pragma unroll
            for (int j = 1; j < 8; j++) {
                qa[j - 1] = L_COMB + (w ^ j) * 6144 + (j & 3) * 1024 + l16; qb[j - 1] = L_W4B + (w ^ j) * 5120 + (j & 3) * 1024 + l16;
                PIN(qa[j - 1]); PIN(qb[j - 1]);
            }
        }
        xo = (RT * 32 + s) * 896 + mem * 128 + kq * 32 + h * 16; PIN(xo);
        STAMP(58);
        f32x16 b0 = zero16(), b1 = zero16();
        sfor<0, 28>([&](auto I) {
            constexpr int i = decltype(I)::v;
            const v4u wv = i < 11 ? wl[i & 3] : W4[i < 11 ? 0 : i - 11];
            if (i & 1) b1 = mfma32(wv, B[i >> 1], b1); else b0 = mfma32(wv, B[i >> 1], b0);
            SB();
            if (i + 4 < 11) wl[i & 3] = w4f(i + 4);
            check(I, B, 1);
            if constexpr (rt == 1) {
                if (i == 11) __syncthreads();
                if constexpr (i >= 16 && i < 20) { t[2 * (i - 13)] = *(LAS f32x4*)(L + qa[i - 13]); t[2 * (i - 13) + 1] = *(LAS f32x4*)(L + qb[i - 13]); }
                if constexpr (i >= 20) add4((i & 1) ? od : e, t[6 + (i - 20)]);
            }
            SB();
        });
        STAMP(19);
#pragma unroll
        for (int g = rt; g < 4; g++) lds_w4f(L, cr + g * 1024, b0, g);
#pragma unroll
        for (int g = rt; g < 4; g++) lds_w4f(L, p5 + g * 1024, b1, g);
        if (rt) { e += f32x4{b0[0], b0[1], b0[2], b0[3]}; od += f32x4{b1[0], b1[1], b1[2], b1[3]}; PIN(e); PIN(od); }
        SB();
        __syncthreads();
        if (rt) {
#pragma unroll
            for (int j = 0; j < 3; j++) { t[2 * j] = *(LAS f32x4*)(L + qa[j]); t[2 * j + 1] = *(LAS f32x4*)(L + qb[j]); }
            SB();
            e = (e + t[0]) + (t[2] + t[4]);
            od = (od + t[1]) + (t[3] + t[5]);
        } else {
#pragma unroll
            for (int j = 0; j < 14; j++) t[j] = *(LAS f32x4*)(L + kq * BLK + l16 + j * 1024);
            SB();
#pragma unroll
            for (int j = 0; j < 14; j++) add4((j & 1) ? od : e, t[j]);
        }
        __builtin_amdgcn_raw_buffer_store_b128(pack8(e, od), __builtin_amdgcn_make_buffer_rsrc((void*)out, 0, S * D * 2, 0x00020000), xo, 0, rt ? OUTAUX : OUTAUX0);
        if (!rt) __builtin_amdgcn_s_barrier();
    }
  };
    if (w < 4) prog(IC<0>{}); else prog(IC<1>{});
    STAMP(20);
}
}

extern "C" void kernel_launch(void* const* d_in, const int* in_sizes, int n_in, void* d_out, int out_size, void* d_ws, size_t ws_size, hipStream_t stream) {
    using hk::u16;
    hk::fused<<<256, 512, 0, stream>>>((const u16*)d_in[0], (const u16*)d_in[1], (const u16*)d_in[2], (const u16*)d_in[3], (const u16*)d_in[5], (const u16*)d_in[6],
                                       (const u16*)d_in[7], (const u16*)d_in[8], (u16*)d_out, (unsigned char*)d_ws);
}
```
